```python
import torch

def _softmax(x, axis=-1):
    return torch.softmax(x, dim=axis)
def _gelu(x):
    return torch.nn.functional.gelu(x, approximate="tanh")
def _silu(x):
    return torch.nn.functional.silu(x)
def _sigmoid(x):
    return torch.sigmoid(x)
def _layernorm(x, axis=-1, eps=1e-5):
    m = x.mean(dim=axis, keepdim=True)
    v = x.var(dim=axis, keepdim=True, unbiased=False)
    return (x - m) / torch.sqrt(v + eps)
def _scatter_add(tgt, idx, src, axis=0):
    return tgt.clone().index_add_(axis, idx, src)
def _segment_sum(src, seg, n):
    out = torch.zeros((n,) + src.shape[1:], dtype=src.dtype)
    return out.index_add_(0, seg, src)
def _pad(x, pw):
    flat = tuple(v for pair in reversed(pw) for v in pair)
    return torch.nn.functional.pad(x, flat)

kernel_name = 'concat_heads__ssm_conv_s4d__ssm_conv_s5__prompt'

def setup_inputs():

    _w = (lambda a: (lambda t: t.to(torch.bfloat16) if t.is_floating_point() else t)(torch.as_tensor(a)))
    def _A_setup():
        Bsz, L, D, N = 1, 2048, 576, 128
        return {
            "u": _w((lambda *s: torch.randn(*s, dtype=torch.bfloat16) * 0.05)(Bsz, L, D)),
            "log_A_real": _w((lambda *s: torch.randn(*s, dtype=torch.bfloat16) * 3.2)(D, N) * 0.5),
            "A_imag": _w((lambda *s: torch.randn(*s, dtype=torch.bfloat16) * 0.05)(D, N) * 3.14),
            "C_real": _w((lambda *s: torch.randn(*s, dtype=torch.bfloat16) * 0.1)(D, N) / N ** 0.5),
            "C_imag": _w((lambda *s: torch.randn(*s, dtype=torch.bfloat16) * 0.1)(D, N) / N ** 0.5),
            "log_dt": _w((lambda *s: torch.randn(*s, dtype=torch.bfloat16) * 0.05)(D) * 0.5 - 4.0),
            "D_skip": _w((lambda *s: torch.randn(*s, dtype=torch.bfloat16) * 0.05)(D)),
        }
    _A_in = _A_setup()

    def _B_setup():
        B, S, D = 1, 2048, 576
        P = D
        Lambda_re = -0.5 * (lambda _x: torch.exp(torch.as_tensor(_x)))((lambda *s: torch.randn(*s, dtype=torch.bfloat16) * 0.2)(P))
        Lambda_im = (lambda *s: torch.randn(*s, dtype=torch.bfloat16) * 0.4)(P) * 1.0
        B_re = (lambda *s: torch.randn(*s, dtype=torch.bfloat16) * 0.2)(P, D) * (D ** -0.5)
        B_im = (lambda *s: torch.randn(*s, dtype=torch.bfloat16) * 0.2)(P, D) * (D ** -0.5)
        C_re = (lambda *s: torch.randn(*s, dtype=torch.bfloat16) * 0.2)(D, P) * (P ** -0.5)
        C_im = (lambda *s: torch.randn(*s, dtype=torch.bfloat16) * 0.2)(D, P) * (P ** -0.5)
        log_dt = (lambda *s: torch.randn(*s, dtype=torch.bfloat16) * 0.2)(P) * 0.3 - 2.0
        Dskip = (lambda *s: torch.randn(*s, dtype=torch.bfloat16) * 0.05)(D)
        return {
            "x": _w((lambda *s: torch.randn(*s, dtype=torch.bfloat16) * 0.05)(B, S, D)),
            "Lambda_re": _w(Lambda_re),
            "Lambda_im": _w(Lambda_im),
            "B_re": _w(B_re),
            "B_im": _w(B_im),
            "C_re": _w(C_re),
            "C_im": _w(C_im),
            "log_dt": _w(log_dt),
            "Dskip": _w(Dskip),
        }
    _B_in = _B_setup()

    return {
        "A_u": _A_in["u"],
        "A_log_A_real": _A_in["log_A_real"],
        "A_A_imag": _A_in["A_imag"],
        "A_C_real": _A_in["C_real"],
        "A_C_imag": _A_in["C_imag"],
        "A_log_dt": _A_in["log_dt"],
        "A_D_skip": _A_in["D_skip"],
        "B_x": _B_in["x"],
        "B_Lambda_re": _B_in["Lambda_re"],
        "B_Lambda_im": _B_in["Lambda_im"],
        "B_B_re": _B_in["B_re"],
        "B_B_im": _B_in["B_im"],
        "B_C_re": _B_in["C_re"],
        "B_C_im": _B_in["C_im"],
        "B_log_dt": _B_in["log_dt"],
        "B_Dskip": _B_in["Dskip"],
    }

def reference(A_u, A_log_A_real, A_A_imag, A_C_real, A_C_imag, A_log_dt, A_D_skip, B_x, B_Lambda_re, B_Lambda_im, B_B_re, B_B_im, B_C_re, B_C_im, B_log_dt, B_Dskip):
    def _A_ref(u, log_A_real, A_imag, C_real, C_imag, log_dt, D_skip):
        L = u.shape[1]
        dt = torch.exp(log_dt)
        A = -torch.exp(log_A_real) + 1j * A_imag
        C = (C_real + 1j * C_imag) * dt[:, None]
        pos = (lambda x, dt: x.to(dt))(torch.arange(L), log_dt.dtype)
        P = torch.exp((A * dt[:, None])[:, :, None] * pos[None, None, :])
        K = 2.0 * torch.einsum("dn,dnl->dl", C, P).real
        L2 = 2 * L
        u_t = (lambda x, *d: x.permute(*d))(u, 0, 2, 1)
        Kf = (lambda x, *a, **k: torch.fft.rfft(x.float(), *a, **k))(K, L2)
        Uf = (lambda x, *a, **k: torch.fft.rfft(x.float(), *a, **k))(u_t, L2)
        y = (lambda x, *a, **k: torch.fft.irfft(x, *a, **k).to(torch.bfloat16))(Uf * Kf[None, :, :], L2)[:, :, :L]
        y = (lambda x, *d: x.permute(*d))(y, 0, 2, 1)
        return y + u * D_skip[None, None, :]

    def _B_ref(x, Lambda_re, Lambda_im, B_re, B_im, C_re, C_im, log_dt, Dskip):
        Bsz, L, H = x.shape
        Lam = Lambda_re + 1j * Lambda_im
        Bmat = B_re + 1j * B_im
        Cmat = C_re + 1j * C_im
        dt = torch.exp(log_dt)
        Lam_bar = torch.exp(dt * Lam)
        B_bar = ((Lam_bar - 1.0) / Lam)[:, None] * Bmat
        Bu = torch.einsum("bld,pd->blp", x + 0j, B_bar)
        h = torch.zeros_like(Bu[:, 0, :])
        hs = []
        for t in range(L):
            h = Lam_bar[None, :] * h + Bu[:, t, :]
            hs.append(h)
        hseq = torch.stack(hs, 1)
        y = 2.0 * torch.einsum("blp,dp->bld", hseq, Cmat).real + x * Dskip[None, None, :]
        return y

    a = _A_ref(A_u, A_log_A_real, A_A_imag, A_C_real, A_C_imag, A_log_dt, A_D_skip)
    b = _B_ref(B_x, B_Lambda_re, B_Lambda_im, B_B_re, B_B_im, B_C_re, B_C_im, B_log_dt, B_Dskip)
    return torch.concatenate([a, b], axis=-1)
```

```cpp
#include <hip/hip_runtime.h>
#include <hip/hip_bf16.h>
#include <stdint.h>

#define L 2048
#define D 576
#define NS 128
#define P 576

typedef unsigned short u16;
__device__ __forceinline__ float bf2f(u16 v){ return __uint_as_float(((unsigned)v)<<16); }
__device__ __forceinline__ float rbf(float f){
    unsigned u = __float_as_uint(f); u = (u + 0x7FFF + ((u>>16)&1)) & 0xFFFF0000u; return __uint_as_float(u);
}

__global__ void kgen(const u16* logA, const u16* Aim, const u16* Cre, const u16* Cim, const u16* logdt, float* K){
    int d = blockIdx.y; int l = blockIdx.x*256 + threadIdx.x;
    float dt = rbf(expf(bf2f(logdt[d])));
    float pos = rbf((float)l);
    float acc = 0.f;
    for(int n=0;n<NS;n++){
        float are = -rbf(expf(bf2f(logA[d*NS+n]))) * dt;
        float aim = bf2f(Aim[d*NS+n]) * dt;
        float cr = bf2f(Cre[d*NS+n])*dt, ci = bf2f(Cim[d*NS+n])*dt;
        float e = expf(are*pos); float s,c; sincosf(aim*pos,&s,&c);
        acc += cr*(e*c) - ci*(e*s);
    }
    K[d*L+l] = 2.f*acc;
}
__global__ void conv(const float* K, const u16* u, const u16* Dskip, float* out){
    int d = blockIdx.x*64 + (threadIdx.x&63); int l = blockIdx.y*4 + (threadIdx.x>>6);
    float acc=0.f;
    for(int j=0;j<=l;j++) acc += K[d*L+j]*bf2f(u[(l-j)*D+d]);
    float y = rbf(acc);
    float s = rbf(bf2f(u[l*D+d])*bf2f(Dskip[d]));
    out[l*(2*D)+d] = rbf(y+s);
}
__global__ void gemm1(const u16* x, const u16* Bre, const u16* Bim, const u16* Lre, const u16* Lim, const u16* logdt, float2* Bu){
    int p = blockIdx.x*64 + (threadIdx.x&63); int l = blockIdx.y*4 + (threadIdx.x>>6);
    float ar=0.f, ai=0.f;
    for(int d=0;d<D;d++){ float xv = bf2f(x[l*D+d]); ar += xv*bf2f(Bre[p*D+d]); ai += xv*bf2f(Bim[p*D+d]); }
    float dt = rbf(expf(bf2f(logdt[p])));
    float lr = bf2f(Lre[p]), li = bf2f(Lim[p]);
    float e = expf(dt*lr); float s,c; sincosf(dt*li,&s,&c);
    float nr = e*c-1.f, ni = e*s;
    float den = lr*lr+li*li;
    float cr = (nr*lr+ni*li)/den, ci = (ni*lr-nr*li)/den;
    Bu[l*P+p] = make_float2(cr*ar-ci*ai, cr*ai+ci*ar);
}
__global__ void scan(float2* Bu, const u16* Lre, const u16* Lim, const u16* logdt){
    int p = blockIdx.x*64 + threadIdx.x;
    float dt = rbf(expf(bf2f(logdt[p])));
    float lr = bf2f(Lre[p]), li = bf2f(Lim[p]);
    float e = expf(dt*lr); float s,c; sincosf(dt*li,&s,&c);
    float ar = e*c, ai = e*s;
    float hr=0.f, hi=0.f;
    for(int t=0;t<L;t++){ float2 b = Bu[t*P+p]; float nr = ar*hr-ai*hi+b.x; float ni = ar*hi+ai*hr+b.y; hr=nr; hi=ni; Bu[t*P+p]=make_float2(hr,hi);}
}
__global__ void gemm2(const float2* h, const u16* Cre, const u16* Cim, const u16* x, const u16* Dskip, float* out){
    int d = blockIdx.x*64 + (threadIdx.x&63); int l = blockIdx.y*4 + (threadIdx.x>>6);
    float acc=0.f;
    for(int p=0;p<P;p++){ float2 hv = h[l*P+p]; acc += hv.x*bf2f(Cre[d*P+p]) - hv.y*bf2f(Cim[d*P+p]); }
    out[l*(2*D)+D+d] = 2.f*acc + rbf(bf2f(x[l*D+d])*bf2f(Dskip[d]));
}

extern "C" void kernel_launch(void* const* d_in, const int* in_sizes, int n_in,
                              void* d_out, int out_size, void* d_ws, size_t ws_size,
                              hipStream_t stream) {
    const u16* u=(const u16*)d_in[0]; const u16* logA=(const u16*)d_in[1]; const u16* Aim=(const u16*)d_in[2];
    const u16* Cre=(const u16*)d_in[3]; const u16* Cim=(const u16*)d_in[4]; const u16* logdt=(const u16*)d_in[5]; const u16* DskA=(const u16*)d_in[6];
    const u16* x=(const u16*)d_in[7]; const u16* Lre=(const u16*)d_in[8]; const u16* Lim=(const u16*)d_in[9];
    const u16* Bre=(const u16*)d_in[10]; const u16* Bim=(const u16*)d_in[11]; const u16* C2re=(const u16*)d_in[12]; const u16* C2im=(const u16*)d_in[13];
    const u16* logdtB=(const u16*)d_in[14]; const u16* DskB=(const u16*)d_in[15];
    float* out=(float*)d_out;
    float* K=(float*)d_ws; float2* Bu=(float2*)((char*)d_ws + (size_t)D*L*4);
    kgen<<<dim3(L/256,D),256,0,stream>>>(logA,Aim,Cre,Cim,logdt,K);
    conv<<<dim3(D/64,L/4),256,0,stream>>>(K,u,DskA,out);
    gemm1<<<dim3(P/64,L/4),256,0,stream>>>(x,Bre,Bim,Lre,Lim,logdtB,Bu);
    scan<<<P/64,64,0,stream>>>(Bu,Lre,Lim,logdtB);
    gemm2<<<dim3(D/64,L/4),256,0,stream>>>(Bu,C2re,C2im,x,DskB,out);
}
```

```cpp
#define PROBE_I0 4
#define PROBE_I1 5
#define PROBE_BLK 200
#define PROBE_AMP 20
#include <hip/hip_runtime.h>
#include <stdint.h>
#define GAS __attribute__((address_space(1)))
#define LAS __attribute__((address_space(3)))
typedef unsigned short u16; typedef unsigned u32; typedef unsigned long long u64;
typedef float f2 __attribute__((ext_vector_type(2)));
typedef float f4 __attribute__((ext_vector_type(4)));
typedef short bf8 __attribute__((ext_vector_type(8)));
typedef u32 u4 __attribute__((ext_vector_type(4)));
typedef u32 u2 __attribute__((ext_vector_type(2)));
typedef __bf16 b2v __attribute__((ext_vector_type(2)));

constexpr int SEQ = 2048, DM = 576, NS = 128;
constexpr size_t WS_AGG = 65536;
constexpr size_t WS_H   = 1u << 20;
constexpr int LDS_BYTES = 160 * 1024;

#define PROBE_DECL u64 pt0_ = 0, pt1_ = 0;
#define STAMP(i) do { if ((i) == PROBE_I0) { pt0_ = __builtin_amdgcn_s_memrealtime(); asm volatile("s_waitcnt lgkmcnt(0)" ::: "memory"); } if ((i) == PROBE_I1) { pt1_ = __builtin_amdgcn_s_memrealtime(); asm volatile("s_waitcnt lgkmcnt(0)" ::: "memory"); } } while (0)
#define PROBE_END do { if (blockIdx.x == PROBE_BLK && w == 0) { const u64 t_ = __builtin_amdgcn_s_memrealtime(), d_ = (pt1_ - pt0_) * PROBE_AMP; for (int k_ = 0; k_ < 2000000 && __builtin_amdgcn_s_memrealtime() - t_ < d_; k_++) __builtin_amdgcn_s_sleep(2); } } while (0)

struct Args {
    const u16 *u, *logA, *Aim, *CreA, *CimA, *logdtA, *DskA;
    const u16 *x, *Lre, *Lim, *Bre, *Bim, *Cre, *Cim, *logdtB, *DskB;
    float* out; unsigned char* ws;
};

__device__ __forceinline__ float bf2f(u16 v) { return __uint_as_float(((u32)v) << 16); }
__device__ __forceinline__ u32 cvtpk(float lo, float hi) { f2 v = {lo, hi}; return __builtin_bit_cast(u32, __builtin_convertvector(v, b2v)); }
__device__ __forceinline__ float rbf(float f) { return __uint_as_float(cvtpk(f, f) << 16); }
__device__ __forceinline__ void bar() { asm volatile("s_barrier" ::: "memory"); }
template <int CTRL> __device__ __forceinline__ float dpp(float v) {
    return __builtin_bit_cast(float, __builtin_amdgcn_update_dpp(0, __builtin_bit_cast(int, v), CTRL, 0xf, 0xf, true));
}
template <int CTRL> __device__ __forceinline__ f2 dpp2(f2 v) { return (f2){dpp<CTRL>(v.x), dpp<CTRL>(v.y)}; }
__device__ __forceinline__ f2 allred16(f2 v) {
    v += dpp2<0x128>(v); v += dpp2<0x124>(v); v += dpp2<0x122>(v); v += dpp2<0x121>(v); return v;
}
#define FMA2(a, b, c) __builtin_elementwise_fma((a), (b), (c))
struct c2 { f2 r, i; };
__device__ __forceinline__ c2 cmul(c2 a, c2 b) { c2 o; o.r = FMA2(a.r, b.r, -(a.i * b.i)); o.i = FMA2(a.r, b.i, a.i * b.r); return o; }
__device__ __forceinline__ c2 cfma(c2 a, c2 b, c2 c) { c2 o; o.r = FMA2(a.r, b.r, FMA2(-a.i, b.i, c.r)); o.i = FMA2(a.r, b.i, FMA2(a.i, b.r, c.i)); return o; }
__device__ __forceinline__ void cpowf(float k, float ar2, float ai2, float& re, float& im) {
    float e = __builtin_amdgcn_exp2f(k * ar2);
    float t = __builtin_amdgcn_fractf(k * ai2);
    re = e * __builtin_amdgcn_cosf(t); im = e * __builtin_amdgcn_sinf(t);
}
__device__ __forceinline__ c2 cpow2(float k, f2 ar2, f2 ai2) {
    float r0, i0, r1, i1; cpowf(k, ar2.x, ai2.x, r0, i0); cpowf(k, ar2.y, ai2.y, r1, i1);
    c2 o; o.r = (f2){r0, r1}; o.i = (f2){i0, i1}; return o;
}

constexpr size_t WS_E    = 0;
constexpr size_t WS_FLAG = 4096;
constexpr int C_KC = 16512, C_COMP = 8256, C_NT = 2064;
constexpr int LDS_MISC = 9 * C_KC;
#define RLX_AGENT __ATOMIC_RELAXED, __HIP_MEMORY_SCOPE_AGENT
constexpr int SPIN_CAP = 100000;

__device__ __forceinline__ void load_c(const Args& a, int j, int lw, int lane, LAS unsigned char* lds, int kc) {
#pragma unroll
    for (int i = 0; i < 4; i++) {
        const int id = lw + 4 * i, comp = id >> 3, row = 8 * (id & 7) + (lane >> 3);
        const unsigned char* base = (const unsigned char*)(comp ? a.Cim : a.Cre);
        const u32 voff = (j * 64 + row) * 1152 + (((lane & 7) ^ ((row >> 1) & 7)) << 4);
        __builtin_amdgcn_global_load_lds((const GAS u32*)(base + kc * 128 + voff), (LAS u32*)(lds + kc * C_KC + id * 1024 + (id >> 1) * 16), 16, 0, 0);
    }
}

__device__ __forceinline__ void b_loader(const Args& a, int c, int g, int lw, int lane, LAS unsigned char* lds) {
    const int t0 = c * 128, p0 = g * 64;
    GAS u64* ep = (GAS u64*)(a.ws + WS_E);
    u64 ev;
    asm volatile("global_load_dwordx2 %0, %1, off sc1" : "=v"(ev) : "v"(ep) : "memory");
    const unsigned char* base[8]; u32 voff[8];
#pragma unroll
    for (int j = 0; j < 8; j++) {
        const int id = lw + 4 * j;
        const int rho = 8 * (id & 15) + (lane >> 3);
        int grow;
        if (j < 4) { grow = p0 + 16 * (rho >> 5) + (rho & 15); base[j] = (const unsigned char*)(((id >> 1) & 1) ? a.Bim : a.Bre); }
        else       { grow = t0 + 8 * (rho & 15) + (rho >> 4);   base[j] = (const unsigned char*)a.x; }
        voff[j] = grow * 1152 + (((lane & 7) ^ ((rho >> 1) & 7)) << 4);
    }
    auto issue = [&](int s) {
#pragma unroll
        for (int j = 0; j < 8; j++)
            __builtin_amdgcn_global_load_lds((const GAS u32*)(base[j] + s * 128 + voff[j]),
                                             (LAS u32*)(lds + (s & 3) * 32768 + (lw + 4 * j) * 1024), 16, 0, 0);
    };
    issue(0); issue(1); issue(2); issue(3);
    asm volatile("s_waitcnt vmcnt(24)" : "+v"(ev) :: "memory");
    const u64 tag = ev + 1;
    if (lw == 0 && lane == 0) *(LAS u64*)(lds + LDS_MISC) = tag;
    asm volatile("s_waitcnt lgkmcnt(0)" ::: "memory");
    bar();
#pragma unroll
    for (int s = 0; s < 9; s++) {
        if (s == 0) asm volatile("s_waitcnt vmcnt(16)" ::: "memory");
        else if (s + 2 <= 8) asm volatile("s_waitcnt vmcnt(8)" ::: "memory");
        else if (s == 7) asm volatile("s_waitcnt vmcnt(8)" ::: "memory");
        bar();
        if (s >= 1 && s + 3 <= 8) issue(s + 3);
        if (s == 6) { load_c(a, g, lw, lane, lds, 8); load_c(a, g, lw, lane, lds, 2); }
        if (s == 7) { load_c(a, g, lw, lane, lds, 3); load_c(a, g, lw, lane, lds, 4); }
        if (s == 8) { load_c(a, g, lw, lane, lds, 5); load_c(a, g, lw, lane, lds, 6); load_c(a, g, lw, lane, lds, 7); }
    }
    bar();
    load_c(a, g, lw, lane, lds, 0); load_c(a, g, lw, lane, lds, 1);
    bar();
    if (lw == 0) {
        const GAS u64* fl = (const GAS u64*)(a.ws + WS_FLAG + (size_t)(c * 9 + (lane < 9 ? lane : 0)) * 128);
        for (int spin = 0; spin < SPIN_CAP; spin++) {
            const u64 v = __hip_atomic_load(fl, RLX_AGENT);
            if (__all(v == tag)) break;
            __builtin_amdgcn_s_sleep(1);
        }
        if (c == 15 && g == 0 && lane == 0) __hip_atomic_store(ep, tag, RLX_AGENT);
    }
    asm volatile("s_waitcnt vmcnt(0)" ::: "memory");
    bar();
}

constexpr int WIN = 12;
__device__ __forceinline__ void b_compute(const Args& a, int c, int g, int w, int lane, LAS unsigned char* lds) {
    PROBE_DECL
    STAMP(0);
    const int n = lane & 15, q = lane >> 4;
    const int pw0 = g * 64 + 16 * w + 4 * q;
    const int pmy = pw0 + (n & 3);
    const u16 r_lr = a.Lre[pmy], r_li = a.Lim[pmy], r_dt = a.logdtB[pmy];
    f4 acc[8][2];
#pragma unroll
    for (int t = 0; t < 8; t++) { acc[t][0] = (f4){0, 0, 0, 0}; acc[t][1] = (f4){0, 0, 0, 0}; }
    const u32 sw = (u32)(n * 128 + ((q ^ ((n >> 1) & 7)) << 4));
    const u32 aW = sw + w * 4096, aX = sw + 16384;
    bf8 fw[2][2], fx[2][8];
    auto rd = [&](int buf, u32 slotoff, int ks) {
        const u32 w_ = (aW ^ (ks << 6)) + slotoff, x_ = (aX ^ (ks << 6)) + slotoff;
        fw[buf][0] = *(const LAS bf8*)(lds + w_); fw[buf][1] = *(const LAS bf8*)(lds + w_ + 2048);
#pragma unroll
        for (int t = 0; t < 8; t++) fx[buf][t] = *(const LAS bf8*)(lds + x_ + t * 2048);
    };
    auto mm = [&](int buf) {
#pragma unroll
        for (int t = 0; t < 8; t++) {
            acc[t][0] = __builtin_amdgcn_mfma_f32_16x16x32_bf16(fw[buf][0], fx[buf][t], acc[t][0], 0, 0, 0);
            acc[t][1] = __builtin_amdgcn_mfma_f32_16x16x32_bf16(fw[buf][1], fx[buf][t], acc[t][1], 0, 0, 0);
        }
    };
    f2 ar2[2], ai2[2]; c2 Lam[2], coef[2], W1[2];
    auto params = [&]() {
        const float lr = bf2f(r_lr), li = bf2f(r_li);
        const float dt = rbf(__builtin_amdgcn_exp2f(bf2f(r_dt) * 1.4426950408889634f));
        const float a2 = (dt * lr) * 1.4426950408889634f, b2 = (dt * li) * 0.15915494309189535f;
        float Lr, Li; cpowf(1.f, a2, b2, Lr, Li);
        const float nr = Lr - 1.f, ni = Li, den = __builtin_amdgcn_rcpf(lr * lr + li * li);
        const float cr = (nr * lr + ni * li) * den, ci = (ni * lr - nr * li) * den;
#define QB(dst, v) { dst[0] = (f2){dpp<0x00>(v), dpp<0x55>(v)}; dst[1] = (f2){dpp<0xAA>(v), dpp<0xFF>(v)}; }
        f2 t_[2];
        QB(ar2, a2); QB(ai2, b2);
        QB(t_, Lr); Lam[0].r = t_[0]; Lam[1].r = t_[1]; QB(t_, Li); Lam[0].i = t_[0]; Lam[1].i = t_[1];
        QB(t_, cr); coef[0].r = t_[0]; coef[1].r = t_[1]; QB(t_, ci); coef[0].i = t_[0]; coef[1].i = t_[1];
        W1[0] = cpow2((float)(8 * (15 - n)), ar2[0], ai2[0]); W1[1] = cpow2((float)(8 * (15 - n)), ar2[1], ai2[1]);
    };
    bar();
    STAMP(1);
    const u64 tag = *(const LAS u64*)(lds + LDS_MISC);
    rd(0, 0, 0);
#define ILV() { _Pragma("unroll") for (int i_ = 0; i_ < 10; i_++) { __builtin_amdgcn_sched_group_barrier(0x8, 1, 0); __builtin_amdgcn_sched_group_barrier(0x100, 1, 0); } __builtin_amdgcn_sched_group_barrier(0x8, 6, 0); }
#pragma unroll
    for (int s = 0; s < 9; s++) {
        bar();
        __builtin_amdgcn_sched_barrier(0);
        rd(1, (s & 3) * 32768, 1);
        mm(0);
        ILV();
        __builtin_amdgcn_sched_barrier(0);
        if (s < 8) rd(0, ((s + 1) & 3) * 32768, 0);
        if (s == 2) params();
        mm(1);
        if (s < 8) ILV();
        __builtin_amdgcn_sched_barrier(0);
    }
    bar();
    STAMP(2);
    const u32 tlo = (u32)tag, thi = (u32)(tag >> 32) & 0xFFu;
    c2 z[8][2];
#pragma unroll
    for (int t = 0; t < 8; t++) {
        z[t][0].r = acc[t][0].xy; z[t][1].r = acc[t][0].zw; z[t][0].i = acc[t][1].xy; z[t][1].i = acc[t][1].zw;
    }
    c2 agg[2];
#pragma unroll
    for (int h = 0; h < 2; h++) agg[h] = z[0][h];
#pragma unroll
    for (int t = 1; t < 8; t++) { agg[0] = cfma(Lam[0], agg[0], z[t][0]); agg[1] = cfma(Lam[1], agg[1], z[t][1]); }
    c2 S[2];
#pragma unroll
    for (int h = 0; h < 2; h++) { S[h] = cmul(W1[h], agg[h]); S[h].r = allred16(S[h].r); S[h].i = allred16(S[h].i); }
    const __amdgpu_buffer_rsrc_t rs = __builtin_amdgcn_make_buffer_rsrc(a.ws, 0, 0x7fffffff, 0x00020000);
    {
        const float sr = (n & 2) ? ((n & 1) ? S[1].r.y : S[1].r.x) : ((n & 1) ? S[0].r.y : S[0].r.x);
        const float si = (n & 2) ? ((n & 1) ? S[1].i.y : S[1].i.x) : ((n & 1) ? S[0].i.y : S[0].i.x);
        u4 gr; gr.x = tlo; gr.y = ((__float_as_uint(sr) + 0x80u) & 0xFFFFFF00u) | thi;
        gr.z = tlo; gr.w = ((__float_as_uint(si) + 0x80u) & 0xFFFFFF00u) | thi;
        if (n < 4) __builtin_amdgcn_raw_buffer_store_b128(gr, rs, (u32)(WS_AGG + ((size_t)c * DM + pw0 + n) * 16), 0, 16);
    }
    STAMP(3);
    const int t0 = c * 128;
    u16 xv[2][4][4]; u16 dsk[4];
#pragma unroll
    for (int nt = 0; nt < 4; nt++) {
        dsk[nt] = a.DskB[g * 64 + nt * 16 + n];
#pragma unroll
        for (int m = 0; m < 2; m++)
#pragma unroll
            for (int r = 0; r < 4; r++) xv[m][nt][r] = a.x[(size_t)(t0 + 8 * (4 * q + r) + 2 * w + m) * DM + g * 64 + nt * 16 + n];
    }
    c2 E[2];
#pragma unroll
    for (int h = 0; h < 2; h++) {
        c2 P = agg[h];
        c2 m = cpow2(8.f, ar2[h], ai2[h]);
        { c2 s; s.r = dpp2<0x111>(P.r); s.i = dpp2<0x111>(P.i); P = cfma(m, s, P); } m = cmul(m, m);
        { c2 s; s.r = dpp2<0x112>(P.r); s.i = dpp2<0x112>(P.i); P = cfma(m, s, P); } m = cmul(m, m);
        { c2 s; s.r = dpp2<0x114>(P.r); s.i = dpp2<0x114>(P.i); P = cfma(m, s, P); } m = cmul(m, m);
        { c2 s; s.r = dpp2<0x118>(P.r); s.i = dpp2<0x118>(P.i); P = cfma(m, s, P); }
        E[h].r = dpp2<0x111>(P.r); E[h].i = dpp2<0x111>(P.i);
    }
    c2 W2[2], PW[2];
#pragma unroll
    for (int h = 0; h < 2; h++) {
        W2[h] = cpow2((float)(128 * (15 - n)), ar2[h], ai2[h]);
        PW[h] = cmul(coef[h], cpow2((float)(8 * n), ar2[h], ai2[h]));
        E[h] = cmul(coef[h], E[h]);
#pragma unroll
        for (int t = 0; t < 8; t++) z[t][h] = cmul(coef[h], z[t][h]);
    }
    u32 skp[2][4][2];
#pragma unroll
    for (int m = 0; m < 2; m++)
#pragma unroll
        for (int nt = 0; nt < 4; nt++) {
            const float d = bf2f(dsk[nt]);
            skp[m][nt][0] = cvtpk(bf2f(xv[m][nt][0]) * d, bf2f(xv[m][nt][1]) * d);
            skp[m][nt][1] = cvtpk(bf2f(xv[m][nt][2]) * d, bf2f(xv[m][nt][3]) * d);
        }
    c2 X[2];
    {
        const int cp = c - 16 + n;
        const bool valid = cp >= 0;
        const u32 off = (u32)(WS_AGG + ((size_t)(valid ? cp : 0) * DM + pw0) * 16);
        u4 g0, g1, g2, g3;
        for (int spin = 0; spin < SPIN_CAP; spin++) {
            g0 = __builtin_amdgcn_raw_buffer_load_b128(rs, off, 0, 16);
            g1 = __builtin_amdgcn_raw_buffer_load_b128(rs, off + 16, 0, 16);
            g2 = __builtin_amdgcn_raw_buffer_load_b128(rs, off + 32, 0, 16);
            g3 = __builtin_amdgcn_raw_buffer_load_b128(rs, off + 48, 0, 16);
            const u32 bad = (g0.x ^ tlo) | (g0.z ^ tlo) | (g1.x ^ tlo) | (g1.z ^ tlo) | (g2.x ^ tlo) | (g2.z ^ tlo) | (g3.x ^ tlo) | (g3.z ^ tlo)
                | (((g0.y ^ thi) | (g0.w ^ thi) | (g1.y ^ thi) | (g1.w ^ thi) | (g2.y ^ thi) | (g2.w ^ thi) | (g3.y ^ thi) | (g3.w ^ thi)) & 0xFFu);
            if (__all(!valid || bad == 0)) break;
            __builtin_amdgcn_s_sleep(1);
        }
        const u32 msk = valid ? 0xFFFFFF00u : 0u;
        c2 Sp[2];
        Sp[0].r = (f2){__uint_as_float(g0.y & msk), __uint_as_float(g1.y & msk)}; Sp[0].i = (f2){__uint_as_float(g0.w & msk), __uint_as_float(g1.w & msk)};
        Sp[1].r = (f2){__uint_as_float(g2.y & msk), __uint_as_float(g3.y & msk)}; Sp[1].i = (f2){__uint_as_float(g2.w & msk), __uint_as_float(g3.w & msk)};
#pragma unroll
        for (int h = 0; h < 2; h++) { X[h] = cmul(W2[h], Sp[h]); X[h].r = allred16(X[h].r); X[h].i = allred16(X[h].i); }
    }
    STAMP(4);
    c2 hst[2];
#pragma unroll
    for (int h = 0; h < 2; h++) hst[h] = cfma(PW[h], X[h], E[h]);
    const u32 hoff = (u32)(WS_H + ((size_t)(c * 36 + g * 4 + w) * 8) * 1024 + lane * 16);
#pragma unroll
    for (int t = 0; t < 8; t++) {
        hst[0] = cfma(Lam[0], hst[0], z[t][0]); hst[1] = cfma(Lam[1], hst[1], z[t][1]);
        u4 o; o.x = cvtpk(hst[0].r.x, hst[0].r.y); o.y = cvtpk(hst[1].r.x, hst[1].r.y);
        o.z = cvtpk(-hst[0].i.x, -hst[0].i.y); o.w = cvtpk(-hst[1].i.x, -hst[1].i.y);
        __builtin_amdgcn_raw_buffer_store_b128(o, rs, hoff + t * 1024, 0, 16);
    }
    asm volatile("s_waitcnt vmcnt(0)" ::: "memory");
    STAMP(5);
    bar();
    if (w == 0 && lane == 0) __hip_atomic_store((GAS u64*)(a.ws + WS_FLAG + (size_t)(c * 9 + g) * 128), tag, RLX_AGENT);
    const u32 hb = (u32)(WS_H + ((size_t)c * 36 * 8 + 2 * w) * 1024 + lane * 16);
    u4 fa[WIN][2];
    auto kcof = [&](int i) { int k = g + i; return k >= 9 ? k - 9 : k; };
    auto ldh = [&](int ci) {
        const u32 so = (u32)(kcof(ci >> 2) * 4 + (ci & 3)) * 8192u;
        fa[ci % WIN][0] = __builtin_amdgcn_raw_buffer_load_b128(rs, hb, so, 16);
        fa[ci % WIN][1] = __builtin_amdgcn_raw_buffer_load_b128(rs, hb + 1024, so, 16);
    };
#pragma unroll
    for (int k = 0; k < 4; k++) ldh(k);
    STAMP(6);
    bar();
    STAMP(7);
#pragma unroll
    for (int k = 4; k < WIN; k++) ldh(k);
    f4 ac2[2][4];
#pragma unroll
    for (int m = 0; m < 2; m++)
#pragma unroll
        for (int nt = 0; nt < 4; nt++) ac2[m][nt] = (f4){0, 0, 0, 0};
    u32 ab[4];
#pragma unroll
    for (int cc = 0; cc < 4; cc++) ab[cc] = (n >> 3) * 1024 + (n & 7) * 128 + ((((2 * cc + (q >> 1)) ^ ((n >> 1) & 7))) << 4) + (q & 1) * 8;
    bf8 fb[2][4];
    auto rdc = [&](int ci) {
        const u32 ad = ab[ci & 3] + (u32)kcof(ci >> 2) * C_KC;
#pragma unroll
        for (int nt = 0; nt < 4; nt++) {
            const u2 re = *(const LAS u2*)(lds + ad + nt * C_NT), im = *(const LAS u2*)(lds + ad + nt * C_NT + C_COMP);
            fb[ci & 1][nt] = __builtin_bit_cast(bf8, (u4){re.x, re.y, im.x, im.y});
        }
    };
    rdc(0);
#pragma unroll
    for (int ci = 0; ci < 36; ci++) {
        __builtin_amdgcn_sched_barrier(0);
        if (ci + 1 < 36) rdc(ci + 1);
        const bf8 a0 = __builtin_bit_cast(bf8, fa[ci % WIN][0]), a1 = __builtin_bit_cast(bf8, fa[ci % WIN][1]);
#pragma unroll
        for (int nt = 0; nt < 4; nt++) {
            ac2[0][nt] = __builtin_amdgcn_mfma_f32_16x16x32_bf16(a0, fb[ci & 1][nt], ac2[0][nt], 0, 0, 0);
            ac2[1][nt] = __builtin_amdgcn_mfma_f32_16x16x32_bf16(a1, fb[ci & 1][nt], ac2[1][nt], 0, 0, 0);
        }
        if (ci + 1 < 36) { _Pragma("unroll") for (int i_ = 0; i_ < 8; i_++) { __builtin_amdgcn_sched_group_barrier(0x8, 1, 0); __builtin_amdgcn_sched_group_barrier(0x100, 1, 0); } }
        __builtin_amdgcn_sched_barrier(0);
        if (ci + WIN < 36) ldh(ci + WIN);
    }
    STAMP(8);
#pragma unroll
    for (int m = 0; m < 2; m++)
#pragma unroll
        for (int r = 0; r < 4; r++)
#pragma unroll
            for (int nt = 0; nt < 4; nt++) {
                const u32 sk = skp[m][nt][r >> 1];
                a.out[(size_t)(t0 + 8 * (4 * q + r) + 2 * w + m) * (2 * DM) + DM + g * 64 + nt * 16 + n] =
                    2.f * ac2[m][nt][r] + __uint_as_float((r & 1) ? (sk & 0xFFFF0000u) : (sk << 16));
            }
    STAMP(9);
    PROBE_END;
}

constexpr int UT_STRIDE = 4640;
constexpr int A_KR = 8 * UT_STRIDE;
constexpr int KR_BYTES = 8704, KV_BYTES = 1344, KR_B = 4160, TROW = 544;
constexpr int A_WAVE = KR_BYTES + KV_BYTES;

__device__ __forceinline__ u32 pk16(u32 lo, u32 hi) { return __builtin_amdgcn_perm(hi, lo, 0x05040100u); }

__device__ __forceinline__ void a_block(const Args& a, int ga, int w, int lane, LAS unsigned char* lds) {
    PROBE_DECL
    STAMP(0);
    const int d0 = ga * 8, d = d0 + w, tid = w * 64 + lane;
    u4 ur[4];
#pragma unroll
    for (int i = 0; i < 4; i++) ur[i] = *(const u4*)(a.u + (size_t)(4 * tid + i) * DM + d0);
    const u32 pA = *(const u32*)(a.logA + d * NS + 2 * lane), pI = *(const u32*)(a.Aim + d * NS + 2 * lane);
    const u32 pR = *(const u32*)(a.CreA + d * NS + 2 * lane), pC = *(const u32*)(a.CimA + d * NS + 2 * lane);
    const float dt = rbf(expf(bf2f(a.logdtA[d]))), dsk = bf2f(a.DskA[d]);
    LAS unsigned char* kr = lds + A_KR + w * A_WAVE;
    LAS unsigned char* kv = kr + KR_BYTES;
    const f2 lA = {__uint_as_float(pA << 16), __uint_as_float(pA & 0xFFFF0000u)};
    const f2 are = {-rbf(expf(lA.x)) * dt, -rbf(expf(lA.y)) * dt};
    const f2 aim = (f2){__uint_as_float(pI << 16), __uint_as_float(pI & 0xFFFF0000u)} * dt;
    const f2 a2 = are * 1.4426950408889634f, b2 = aim * 0.15915494309189535f;
    c2 Cc; Cc.r = (f2){__uint_as_float(pR << 16), __uint_as_float(pR & 0xFFFF0000u)} * dt * 2.f;
    Cc.i = (f2){__uint_as_float(pC << 16), __uint_as_float(pC & 0xFFFF0000u)} * dt * 2.f;
    const c2 w1 = cpow2(1.f, a2, b2), w8 = cpow2(8.f, a2, b2), w128 = cpow2(128.f, a2, b2);
    const c2 w2 = cmul(w1, w1), w4 = cmul(w2, w2);
    auto put = [&](int row, c2 v) { *(LAS u2*)(kr + row * TROW + lane * 8) = (u2){cvtpk(v.r.x, v.i.x), cvtpk(v.r.y, v.i.y)}; };
    const int jx = lane & 15, g4 = lane >> 4;
    const u32 fra = jx * TROW + g4 * 16;
    bf8 fr[8];
    {
        c2 v; v.r = (f2){1.f, 1.f}; v.i = (f2){0.f, 0.f};
#pragma unroll
        for (int j = 0; j < 16; j++) { c2 t; t.r = v.r; t.i = -v.i; put(j, t); if (j < 15) v = cmul(v, w8); }
#pragma unroll
        for (int s = 0; s < 8; s++) fr[s] = *(const LAS bf8*)(kr + fra + s * 64);
    }
    auto tile = [&]() {
        f4 acc = {0, 0, 0, 0};
#pragma unroll
        for (int s = 0; s < 8; s++) acc = __builtin_amdgcn_mfma_f32_16x16x32_bf16(*(const LAS bf8*)(kr + fra + s * 64), fr[s], acc, 0, 0, 0);
        return acc;
    };
    c2 G = Cc;
    {
        asm volatile("s_waitcnt lgkmcnt(0)" ::: "memory");
#pragma unroll
        for (int i = 0; i < 2; i++) {
            c2 v = G;
#pragma unroll
            for (int e = 0; e < 8; e++) { put(8 * i + e, v); if (e < 7) v = cmul(v, w1); }
            G = cmul(G, w128);
        }
        const f4 k = tile();
        *(LAS u2*)(kv + 2 * (128 * (g4 >> 1) + 8 * jx + 4 * (g4 & 1))) = (u2){cvtpk(k[0], k[1]), cvtpk(k[2], k[3])};
    }
    {
        asm volatile("s_waitcnt lgkmcnt(0)" ::: "memory");
#pragma unroll
        for (int i = 0; i < 2; i++) {
            c2 v = G;
#pragma unroll
            for (int e = 0; e < 4; e++) { put(4 * i + e, v); if (e < 3) v = cmul(v, w2); }
            G = cmul(G, w128);
        }
#pragma unroll
        for (int i = 0; i < 4; i++) { put(8 + 2 * i, G); put(9 + 2 * i, cmul(G, w4)); G = cmul(G, w128); }
        const f4 k = tile();
        const int i0 = g4 < 2 ? 256 + 64 * g4 + 4 * jx : 384 + 64 * (g4 - 2) + 2 * jx, i1 = i0 + (g4 < 2 ? 2 : 32);
        *(LAS u32*)(kv + 2 * i0) = cvtpk(k[0], k[1]); *(LAS u32*)(kv + 2 * i1) = cvtpk(k[2], k[3]);
    }
    {
        asm volatile("s_waitcnt lgkmcnt(0)" ::: "memory");
#pragma unroll
        for (int i = 0; i < 9; i++) { put(i, G); if (i < 8) G = cmul(G, w128); }
        const f4 k = tile();
        const u32 k01 = cvtpk(k[0], k[1]), k23 = cvtpk(k[2], k[3]);
        if (g4 < 2) {
            LAS u16* p = (LAS u16*)(kv + 2 * (512 + 64 * g4 + jx));
            p[0] = (u16)k01; p[16] = (u16)(k01 >> 16); p[32] = (u16)k23; p[48] = (u16)(k23 >> 16);
        } else if (lane == 32) *(LAS u16*)(kv + 2 * 640) = (u16)k01;
    }
#pragma unroll
    for (int j = 0; j < 4; j++) {
        const u32 r0 = ur[0][j], r1 = ur[1][j], r2 = ur[2][j], r3 = ur[3][j];
        *(LAS u2*)(lds + (2 * j) * UT_STRIDE + 512 + 8 * tid) = (u2){__builtin_amdgcn_perm(r1, r0, 0x05040100u), __builtin_amdgcn_perm(r3, r2, 0x05040100u)};
        *(LAS u2*)(lds + (2 * j + 1) * UT_STRIDE + 512 + 8 * tid) = (u2){__builtin_amdgcn_perm(r1, r0, 0x07060302u), __builtin_amdgcn_perm(r3, r2, 0x07060302u)};
    }
    if (lane < 32) *(LAS u4*)(lds + w * UT_STRIDE + lane * 16) = (u4){0, 0, 0, 0};
    asm volatile("s_waitcnt lgkmcnt(0)" ::: "memory");
    {
        const LAS u16* kvp = (const LAS u16*)kv;
        u32 prev7 = 0;
#pragma unroll
        for (int qq = 0; qq < 4; qq++) {
            const int c = 64 * qq + lane;
            u32 k[8];
            if (qq == 0) {
                const bool s1 = lane >= 32;
                const int base = s1 ? 256 + 4 * (lane - 32) : 8 * lane;
                const int o1[8] = {0, 0, 1, 2, 2, 2, 3, 4};
#pragma unroll
                for (int i = 0; i < 8; i++) k[i] = kvp[base + (s1 ? o1[i] : i)];
            } else if (qq == 1) {
                const int base = 384 + 2 * lane;
                const u32 v0 = kvp[base], v1 = kvp[base + 1], v2 = kvp[base + 2];
                k[0] = k[1] = k[2] = v0; k[3] = k[4] = k[5] = v1; k[6] = k[7] = v2;
            } else {
                const int base = 512 + (c - 128);
                const u32 v0 = kvp[base], v1 = kvp[base + 1];
                k[0] = k[1] = k[2] = k[3] = v0; k[4] = (lane & 1) ? v1 : v0; k[5] = k[6] = k[7] = v1;
            }
            const u32 km1 = (u32)__builtin_amdgcn_update_dpp((int)prev7, (int)k[7], 0x138, 0xf, 0xf, false);
            prev7 = (u32)__builtin_amdgcn_readlane((int)k[7], 63);
            *(LAS u4*)(kr + 16 * (255 - c)) = (u4){pk16(k[7], k[6]), pk16(k[5], k[4]), pk16(k[3], k[2]), pk16(k[1], k[0])};
            *(LAS u4*)(kr + KR_B + 16 * (255 - c)) = (u4){pk16(k[6], k[5]), pk16(k[4], k[3]), pk16(k[2], k[1]), pk16(k[0], km1)};
        }
        if (lane < 2) { *(LAS u4*)(kr + 4096 + lane * 16) = (u4){0, 0, 0, 0}; *(LAS u4*)(kr + KR_B + 4096 + lane * 16) = (u4){0, 0, 0, 0}; }
    }
    STAMP(1);
    asm volatile("s_waitcnt lgkmcnt(0)" ::: "memory");
    bar();
    STAMP(2);
    f4 acc[8];
#pragma unroll
    for (int t = 0; t < 8; t++) acc[t] = (f4){0, 0, 0, 0};
    {
        const int b = jx, kk = g4 >> 1, eh = g4 & 1;
        u32 ka = (u32)(A_KR + w * A_WAVE) + ((b & 1) ? 2 * (2047 - 16 * kk + 8 * eh - b) : KR_B + 2 * (2046 - 16 * kk + 8 * eh - b)) - 3584;
        u32 ua = (u32)(w * UT_STRIDE) + 512 + 32 * jx - 32 * kk + 16 * eh;
        for (int r = 0; r < 8; r++) {
            bf8 fu[8];
#pragma unroll
            for (int t = 0; t < 8; t++) fu[t] = *(const LAS bf8*)(lds + ua + 512 * t);
#pragma unroll
            for (int i = 0; i < 8; i++) {
                const LAS u32* p = (const LAS u32*)(lds + ka + 512 * (7 - i));
                const bf8 fk = __builtin_bit_cast(bf8, (u4){p[0], p[1], p[2], p[3]});
#pragma unroll
                for (int t = i; t < 8; t++) acc[t] = __builtin_amdgcn_mfma_f32_16x16x32_bf16(fk, fu[t - i], acc[t], 0, 0, 0);
            }
            ka -= 64; ua -= 64;
        }
    }
    STAMP(3);
#pragma unroll
    for (int t = 0; t < 8; t++) {
        LAS u2* p = (LAS u2*)(lds + w * UT_STRIDE + 512 + 2 * (256 * t + 16 * jx + 4 * g4));
        const u2 uu = *p;
        const u32 y01 = cvtpk(acc[t][0], acc[t][1]), y23 = cvtpk(acc[t][2], acc[t][3]);
        const u32 s01 = cvtpk(__uint_as_float(uu.x << 16) * dsk, __uint_as_float(uu.x & 0xFFFF0000u) * dsk);
        const u32 s23 = cvtpk(__uint_as_float(uu.y << 16) * dsk, __uint_as_float(uu.y & 0xFFFF0000u) * dsk);
        *p = (u2){cvtpk(__uint_as_float(y01 << 16) + __uint_as_float(s01 << 16), __uint_as_float(y01 & 0xFFFF0000u) + __uint_as_float(s01 & 0xFFFF0000u)),
                  cvtpk(__uint_as_float(y23 << 16) + __uint_as_float(s23 << 16), __uint_as_float(y23 & 0xFFFF0000u) + __uint_as_float(s23 & 0xFFFF0000u))};
    }
    asm volatile("s_waitcnt lgkmcnt(0)" ::: "memory");
    bar();
    STAMP(4);
    const int hf = tid & 1;
#pragma unroll
    for (int ps = 0; ps < 2; ps++) {
        const int rq = (tid >> 1) + 256 * ps;
        u2 v[4];
#pragma unroll
        for (int j = 0; j < 4; j++) v[j] = *(const LAS u2*)(lds + (4 * hf + j) * UT_STRIDE + 512 + 8 * rq);
        float* o = a.out + (size_t)(4 * rq) * (2 * DM) + d0 + 4 * hf;
        *(f4*)(o) = (f4){__uint_as_float(v[0].x << 16), __uint_as_float(v[1].x << 16), __uint_as_float(v[2].x << 16), __uint_as_float(v[3].x << 16)};
        *(f4*)(o + 2 * DM) = (f4){__uint_as_float(v[0].x & 0xFFFF0000u), __uint_as_float(v[1].x & 0xFFFF0000u), __uint_as_float(v[2].x & 0xFFFF0000u), __uint_as_float(v[3].x & 0xFFFF0000u)};
        *(f4*)(o + 4 * DM) = (f4){__uint_as_float(v[0].y << 16), __uint_as_float(v[1].y << 16), __uint_as_float(v[2].y << 16), __uint_as_float(v[3].y << 16)};
        *(f4*)(o + 6 * DM) = (f4){__uint_as_float(v[0].y & 0xFFFF0000u), __uint_as_float(v[1].y & 0xFFFF0000u), __uint_as_float(v[2].y & 0xFFFF0000u), __uint_as_float(v[3].y & 0xFFFF0000u)};
    }
    STAMP(5);
    PROBE_END;
}

__global__ __launch_bounds__(512, 2) void kAll(Args a) {
    extern __shared__ __attribute__((aligned(16))) unsigned char smem[];
    LAS unsigned char* lds = (LAS unsigned char*)smem;
    const int b = blockIdx.x, xcd = b & 7, i = b >> 3;
    const int lane = threadIdx.x & 63, w = __builtin_amdgcn_readfirstlane(threadIdx.x >> 6);
    if (i < 18) {
        const int c = 2 * xcd + (i >= 9), g = i % 9;
        if (w < 4) b_compute(a, c, g, w, lane, lds);
        else b_loader(a, c, g, w - 4, lane, lds);
    } else a_block(a, 9 * xcd + (i - 18), w, lane, lds);
}

extern "C" void kernel_launch(void* const* d_in, const int* in_sizes, int n_in, void* d_out, int out_size, void* d_ws, size_t ws_size, hipStream_t stream) {
    Args a;
    a.u = (const u16*)d_in[0]; a.logA = (const u16*)d_in[1]; a.Aim = (const u16*)d_in[2]; a.CreA = (const u16*)d_in[3]; a.CimA = (const u16*)d_in[4];
    a.logdtA = (const u16*)d_in[5]; a.DskA = (const u16*)d_in[6];
    a.x = (const u16*)d_in[7]; a.Lre = (const u16*)d_in[8]; a.Lim = (const u16*)d_in[9]; a.Bre = (const u16*)d_in[10]; a.Bim = (const u16*)d_in[11];
    a.Cre = (const u16*)d_in[12]; a.Cim = (const u16*)d_in[13]; a.logdtB = (const u16*)d_in[14]; a.DskB = (const u16*)d_in[15];
    a.out = (float*)d_out; a.ws = (unsigned char*)d_ws;
    (void)hipFuncSetAttribute((const void*)kAll, hipFuncAttributeMaxDynamicSharedMemorySize, LDS_BYTES);
    kAll<<<216, 512, LDS_BYTES, stream>>>(a);
}
```

```cpp
#define PROBE2_Q 0
#define PROBE_BLK 100
#define PROBE_AMP 5
#include <hip/hip_runtime.h>
#include <stdint.h>
#define GAS __attribute__((address_space(1)))
#define LAS __attribute__((address_space(3)))
typedef unsigned short u16; typedef unsigned u32; typedef unsigned long long u64;
typedef float f2 __attribute__((ext_vector_type(2)));
typedef float f4 __attribute__((ext_vector_type(4)));
typedef short bf8 __attribute__((ext_vector_type(8)));
typedef u32 u4 __attribute__((ext_vector_type(4)));
typedef u32 u2 __attribute__((ext_vector_type(2)));
typedef __bf16 b2v __attribute__((ext_vector_type(2)));

constexpr int SEQ = 2048, DM = 576, NS = 128;
constexpr size_t WS_AGG = 65536;
constexpr size_t WS_H   = 1u << 20;
constexpr int LDS_BYTES = 160 * 1024;

#define PROBE_DECL u64 pt0_ = 0;
#define STAMP(i) do { if ((i) == 0) { pt0_ = __builtin_amdgcn_s_memrealtime(); if (w == 0 && lane == 0) __hip_atomic_store((GAS u64*)(a.ws + (64u << 20) + blockIdx.x * 16), pt0_, __ATOMIC_RELAXED, __HIP_MEMORY_SCOPE_AGENT); } } while (0)
#define PROBE_END do { if (w == 0) { const u64 te_ = __builtin_amdgcn_s_memrealtime(); \
    if (lane == 0) __hip_atomic_store((GAS u64*)(a.ws + (64u << 20) + blockIdx.x * 16 + 8), te_, __ATOMIC_RELAXED, __HIP_MEMORY_SCOPE_AGENT); \
    if (blockIdx.x == PROBE_BLK) { \
      for (int k_ = 0; k_ < 2000000 && __builtin_amdgcn_s_memrealtime() - pt0_ < 4000; k_++) __builtin_amdgcn_s_sleep(2); \
      long long mn_ = 1ll << 40, mx_ = -(1ll << 40); \
      for (int j_ = 0; j_ < 4; j_++) { int bb_ = lane + 64 * j_; if (bb_ < 216) { \
        long long s_ = (long long)(__hip_atomic_load((GAS u64*)(a.ws + (64u << 20) + bb_ * 16), __ATOMIC_RELAXED, __HIP_MEMORY_SCOPE_AGENT) - pt0_); \
        long long e_ = (long long)(__hip_atomic_load((GAS u64*)(a.ws + (64u << 20) + bb_ * 16 + 8), __ATOMIC_RELAXED, __HIP_MEMORY_SCOPE_AGENT) - pt0_); \
        if (s_ > -3000 && s_ < 4000 && s_ < mn_) mn_ = s_; if (e_ > -3000 && e_ < 4000 && e_ > mx_) mx_ = e_; } } \
      for (int o_ = 32; o_ >= 1; o_ >>= 1) { long long m1_ = __shfl_xor(mn_, o_), m2_ = __shfl_xor(mx_, o_); mn_ = m1_ < mn_ ? m1_ : mn_; mx_ = m2_ > mx_ ? m2_ : mx_; } \
      const long long q_ = PROBE2_Q == 0 ? 0 : PROBE2_Q == 1 ? mx_ - mn_ : PROBE2_Q == 2 ? -mn_ : mx_; \
      const u64 t_ = __builtin_amdgcn_s_memrealtime(), d_ = (u64)(q_ * PROBE_AMP); \
      for (int k_ = 0; k_ < 2000000 && __builtin_amdgcn_s_memrealtime() - t_ < d_; k_++) __builtin_amdgcn_s_sleep(2); } } } while (0)

struct Args {
    const u16 *u, *logA, *Aim, *CreA, *CimA, *logdtA, *DskA;
    const u16 *x, *Lre, *Lim, *Bre, *Bim, *Cre, *Cim, *logdtB, *DskB;
    float* out; unsigned char* ws;
};

__device__ __forceinline__ float bf2f(u16 v) { return __uint_as_float(((u32)v) << 16); }
__device__ __forceinline__ u32 cvtpk(float lo, float hi) { f2 v = {lo, hi}; return __builtin_bit_cast(u32, __builtin_convertvector(v, b2v)); }
__device__ __forceinline__ float rbf(float f) { return __uint_as_float(cvtpk(f, f) << 16); }
__device__ __forceinline__ void bar() { asm volatile("s_barrier" ::: "memory"); }
template <int CTRL> __device__ __forceinline__ float dpp(float v) {
    return __builtin_bit_cast(float, __builtin_amdgcn_update_dpp(0, __builtin_bit_cast(int, v), CTRL, 0xf, 0xf, true));
}
template <int CTRL> __device__ __forceinline__ f2 dpp2(f2 v) { return (f2){dpp<CTRL>(v.x), dpp<CTRL>(v.y)}; }
__device__ __forceinline__ f2 allred16(f2 v) {
    v += dpp2<0x128>(v); v += dpp2<0x124>(v); v += dpp2<0x122>(v); v += dpp2<0x121>(v); return v;
}
#define FMA2(a, b, c) __builtin_elementwise_fma((a), (b), (c))
struct c2 { f2 r, i; };
__device__ __forceinline__ c2 cmul(c2 a, c2 b) { c2 o; o.r = FMA2(a.r, b.r, -(a.i * b.i)); o.i = FMA2(a.r, b.i, a.i * b.r); return o; }
__device__ __forceinline__ c2 cfma(c2 a, c2 b, c2 c) { c2 o; o.r = FMA2(a.r, b.r, FMA2(-a.i, b.i, c.r)); o.i = FMA2(a.r, b.i, FMA2(a.i, b.r, c.i)); return o; }
__device__ __forceinline__ void cpowf(float k, float ar2, float ai2, float& re, float& im) {
    float e = __builtin_amdgcn_exp2f(k * ar2);
    float t = __builtin_amdgcn_fractf(k * ai2);
    re = e * __builtin_amdgcn_cosf(t); im = e * __builtin_amdgcn_sinf(t);
}
__device__ __forceinline__ c2 cpow2(float k, f2 ar2, f2 ai2) {
    float r0, i0, r1, i1; cpowf(k, ar2.x, ai2.x, r0, i0); cpowf(k, ar2.y, ai2.y, r1, i1);
    c2 o; o.r = (f2){r0, r1}; o.i = (f2){i0, i1}; return o;
}

constexpr size_t WS_E    = 0;
constexpr size_t WS_FLAG = 4096;
constexpr int C_KC = 16512, C_COMP = 8256, C_NT = 2064;
constexpr int LDS_MISC = 9 * C_KC;
#define RLX_AGENT __ATOMIC_RELAXED, __HIP_MEMORY_SCOPE_AGENT
constexpr int SPIN_CAP = 100000;

__device__ __forceinline__ void load_c(const Args& a, int j, int lw, int lane, LAS unsigned char* lds, int kc) {
#pragma unroll
    for (int i = 0; i < 4; i++) {
        const int id = lw + 4 * i, comp = id >> 3, row = 8 * (id & 7) + (lane >> 3);
        const unsigned char* base = (const unsigned char*)(comp ? a.Cim : a.Cre);
        const u32 voff = (j * 64 + row) * 1152 + (((lane & 7) ^ ((row >> 1) & 7)) << 4);
        __builtin_amdgcn_global_load_lds((const GAS u32*)(base + kc * 128 + voff), (LAS u32*)(lds + kc * C_KC + id * 1024 + (id >> 1) * 16), 16, 0, 0);
    }
}

__device__ __forceinline__ void b_loader(const Args& a, int c, int g, int lw, int lane, LAS unsigned char* lds) {
    const int t0 = c * 128, p0 = g * 64;
    GAS u64* ep = (GAS u64*)(a.ws + WS_E);
    u64 ev;
    asm volatile("global_load_dwordx2 %0, %1, off sc1" : "=v"(ev) : "v"(ep) : "memory");
    const unsigned char* base[8]; u32 voff[8];
#pragma unroll
    for (int j = 0; j < 8; j++) {
        const int id = lw + 4 * j;
        const int rho = 8 * (id & 15) + (lane >> 3);
        int grow;
        if (j < 4) { grow = p0 + 16 * (rho >> 5) + (rho & 15); base[j] = (const unsigned char*)(((id >> 1) & 1) ? a.Bim : a.Bre); }
        else       { grow = t0 + 8 * (rho & 15) + (rho >> 4);   base[j] = (const unsigned char*)a.x; }
        voff[j] = grow * 1152 + (((lane & 7) ^ ((rho >> 1) & 7)) << 4);
    }
    auto issue = [&](int s) {
#pragma unroll
        for (int j = 0; j < 8; j++)
            __builtin_amdgcn_global_load_lds((const GAS u32*)(base[j] + s * 128 + voff[j]),
                                             (LAS u32*)(lds + (s & 3) * 32768 + (lw + 4 * j) * 1024), 16, 0, 0);
    };
    issue(0); issue(1); issue(2); issue(3);
    asm volatile("s_waitcnt vmcnt(24)" : "+v"(ev) :: "memory");
    const u64 tag = ev + 1;
    if (lw == 0 && lane == 0) *(LAS u64*)(lds + LDS_MISC) = tag;
    asm volatile("s_waitcnt lgkmcnt(0)" ::: "memory");
    bar();
#pragma unroll
    for (int s = 0; s < 9; s++) {
        if (s == 0) asm volatile("s_waitcnt vmcnt(16)" ::: "memory");
        else if (s + 2 <= 8) asm volatile("s_waitcnt vmcnt(8)" ::: "memory");
        else if (s == 7) asm volatile("s_waitcnt vmcnt(8)" ::: "memory");
        bar();
        if (s >= 1 && s + 3 <= 8) issue(s + 3);
        if (s == 6) { load_c(a, g, lw, lane, lds, 8); load_c(a, g, lw, lane, lds, 2); }
        if (s == 7) { load_c(a, g, lw, lane, lds, 3); load_c(a, g, lw, lane, lds, 4); }
        if (s == 8) { load_c(a, g, lw, lane, lds, 5); load_c(a, g, lw, lane, lds, 6); load_c(a, g, lw, lane, lds, 7); }
    }
    bar();
    load_c(a, g, lw, lane, lds, 0); load_c(a, g, lw, lane, lds, 1);
    bar();
    if (lw == 0) {
        const GAS u64* fl = (const GAS u64*)(a.ws + WS_FLAG + (size_t)(c * 9 + (lane < 9 ? lane : 0)) * 128);
        for (int spin = 0; spin < SPIN_CAP; spin++) {
            const u64 v = __hip_atomic_load(fl, RLX_AGENT);
            if (__all(v == tag)) break;
            __builtin_amdgcn_s_sleep(1);
        }
        if (c == 15 && g == 0 && lane == 0) __hip_atomic_store(ep, tag, RLX_AGENT);
    }
    asm volatile("s_waitcnt vmcnt(0)" ::: "memory");
    bar();
}

constexpr int WIN = 12;
__device__ __forceinline__ void b_compute(const Args& a, int c, int g, int w, int lane, LAS unsigned char* lds) {
    PROBE_DECL
    STAMP(0);
    const int n = lane & 15, q = lane >> 4;
    const int pw0 = g * 64 + 16 * w + 4 * q;
    const int pmy = pw0 + (n & 3);
    const u16 r_lr = a.Lre[pmy], r_li = a.Lim[pmy], r_dt = a.logdtB[pmy];
    f4 acc[8][2];
#pragma unroll
    for (int t = 0; t < 8; t++) { acc[t][0] = (f4){0, 0, 0, 0}; acc[t][1] = (f4){0, 0, 0, 0}; }
    const u32 sw = (u32)(n * 128 + ((q ^ ((n >> 1) & 7)) << 4));
    const u32 aW = sw + w * 4096, aX = sw + 16384;
    bf8 fw[2][2], fx[2][8];
    auto rd = [&](int buf, u32 slotoff, int ks) {
        const u32 w_ = (aW ^ (ks << 6)) + slotoff, x_ = (aX ^ (ks << 6)) + slotoff;
        fw[buf][0] = *(const LAS bf8*)(lds + w_); fw[buf][1] = *(const LAS bf8*)(lds + w_ + 2048);
#pragma unroll
        for (int t = 0; t < 8; t++) fx[buf][t] = *(const LAS bf8*)(lds + x_ + t * 2048);
    };
    auto mm = [&](int buf) {
#pragma unroll
        for (int t = 0; t < 8; t++) {
            acc[t][0] = __builtin_amdgcn_mfma_f32_16x16x32_bf16(fw[buf][0], fx[buf][t], acc[t][0], 0, 0, 0);
            acc[t][1] = __builtin_amdgcn_mfma_f32_16x16x32_bf16(fw[buf][1], fx[buf][t], acc[t][1], 0, 0, 0);
        }
    };
    f2 ar2[2], ai2[2]; c2 Lam[2], coef[2], W1[2];
    auto params = [&]() {
        const float lr = bf2f(r_lr), li = bf2f(r_li);
        const float dt = rbf(__builtin_amdgcn_exp2f(bf2f(r_dt) * 1.4426950408889634f));
        const float a2 = (dt * lr) * 1.4426950408889634f, b2 = (dt * li) * 0.15915494309189535f;
        float Lr, Li; cpowf(1.f, a2, b2, Lr, Li);
        const float nr = Lr - 1.f, ni = Li, den = __builtin_amdgcn_rcpf(lr * lr + li * li);
        const float cr = (nr * lr + ni * li) * den, ci = (ni * lr - nr * li) * den;
#define QB(dst, v) { dst[0] = (f2){dpp<0x00>(v), dpp<0x55>(v)}; dst[1] = (f2){dpp<0xAA>(v), dpp<0xFF>(v)}; }
        f2 t_[2];
        QB(ar2, a2); QB(ai2, b2);
        QB(t_, Lr); Lam[0].r = t_[0]; Lam[1].r = t_[1]; QB(t_, Li); Lam[0].i = t_[0]; Lam[1].i = t_[1];
        QB(t_, cr); coef[0].r = t_[0]; coef[1].r = t_[1]; QB(t_, ci); coef[0].i = t_[0]; coef[1].i = t_[1];
        W1[0] = cpow2((float)(8 * (15 - n)), ar2[0], ai2[0]); W1[1] = cpow2((float)(8 * (15 - n)), ar2[1], ai2[1]);
    };
    bar();
    STAMP(1);
    const u64 tag = *(const LAS u64*)(lds + LDS_MISC);
    rd(0, 0, 0);
#define ILV() { _Pragma("unroll") for (int i_ = 0; i_ < 10; i_++) { __builtin_amdgcn_sched_group_barrier(0x8, 1, 0); __builtin_amdgcn_sched_group_barrier(0x100, 1, 0); } __builtin_amdgcn_sched_group_barrier(0x8, 6, 0); }
#pragma unroll
    for (int s = 0; s < 9; s++) {
        bar();
        __builtin_amdgcn_sched_barrier(0);
        rd(1, (s & 3) * 32768, 1);
        mm(0);
        ILV();
        __builtin_amdgcn_sched_barrier(0);
        if (s < 8) rd(0, ((s + 1) & 3) * 32768, 0);
        if (s == 2) params();
        mm(1);
        if (s < 8) ILV();
        __builtin_amdgcn_sched_barrier(0);
    }
    bar();
    STAMP(2);
    const u32 tlo = (u32)tag, thi = (u32)(tag >> 32) & 0xFFu;
    c2 z[8][2];
#pragma unroll
    for (int t = 0; t < 8; t++) {
        z[t][0].r = acc[t][0].xy; z[t][1].r = acc[t][0].zw; z[t][0].i = acc[t][1].xy; z[t][1].i = acc[t][1].zw;
    }
    c2 agg[2];
#pragma unroll
    for (int h = 0; h < 2; h++) agg[h] = z[0][h];
#pragma unroll
    for (int t = 1; t < 8; t++) { agg[0] = cfma(Lam[0], agg[0], z[t][0]); agg[1] = cfma(Lam[1], agg[1], z[t][1]); }
    c2 S[2];
#pragma unroll
    for (int h = 0; h < 2; h++) { S[h] = cmul(W1[h], agg[h]); S[h].r = allred16(S[h].r); S[h].i = allred16(S[h].i); }
    const __amdgpu_buffer_rsrc_t rs = __builtin_amdgcn_make_buffer_rsrc(a.ws, 0, 0x7fffffff, 0x00020000);
    {
        const float sr = (n & 2) ? ((n & 1) ? S[1].r.y : S[1].r.x) : ((n & 1) ? S[0].r.y : S[0].r.x);
        const float si = (n & 2) ? ((n & 1) ? S[1].i.y : S[1].i.x) : ((n & 1) ? S[0].i.y : S[0].i.x);
        u4 gr; gr.x = tlo; gr.y = ((__float_as_uint(sr) + 0x80u) & 0xFFFFFF00u) | thi;
        gr.z = tlo; gr.w = ((__float_as_uint(si) + 0x80u) & 0xFFFFFF00u) | thi;
        if (n < 4) __builtin_amdgcn_raw_buffer_store_b128(gr, rs, (u32)(WS_AGG + ((size_t)c * DM + pw0 + n) * 16), 0, 16);
    }
    STAMP(3);
    const int t0 = c * 128;
    u16 xv[2][4][4]; u16 dsk[4];
#pragma unroll
    for (int nt = 0; nt < 4; nt++) {
        dsk[nt] = a.DskB[g * 64 + nt * 16 + n];
#pragma unroll
        for (int m = 0; m < 2; m++)
#pragma unroll
            for (int r = 0; r < 4; r++) xv[m][nt][r] = a.x[(size_t)(t0 + 8 * (4 * q + r) + 2 * w + m) * DM + g * 64 + nt * 16 + n];
    }
    c2 E[2];
#pragma unroll
    for (int h = 0; h < 2; h++) {
        c2 P = agg[h];
        c2 m = cpow2(8.f, ar2[h], ai2[h]);
        { c2 s; s.r = dpp2<0x111>(P.r); s.i = dpp2<0x111>(P.i); P = cfma(m, s, P); } m = cmul(m, m);
        { c2 s; s.r = dpp2<0x112>(P.r); s.i = dpp2<0x112>(P.i); P = cfma(m, s, P); } m = cmul(m, m);
        { c2 s; s.r = dpp2<0x114>(P.r); s.i = dpp2<0x114>(P.i); P = cfma(m, s, P); } m = cmul(m, m);
        { c2 s; s.r = dpp2<0x118>(P.r); s.i = dpp2<0x118>(P.i); P = cfma(m, s, P); }
        E[h].r = dpp2<0x111>(P.r); E[h].i = dpp2<0x111>(P.i);
    }
    c2 W2[2], PW[2];
#pragma unroll
    for (int h = 0; h < 2; h++) {
        W2[h] = cpow2((float)(128 * (15 - n)), ar2[h], ai2[h]);
        PW[h] = cmul(coef[h], cpow2((float)(8 * n), ar2[h], ai2[h]));
        E[h] = cmul(coef[h], E[h]);
#pragma unroll
        for (int t = 0; t < 8; t++) z[t][h] = cmul(coef[h], z[t][h]);
    }
    u32 skp[2][4][2];
#pragma unroll
    for (int m = 0; m < 2; m++)
#pragma unroll
        for (int nt = 0; nt < 4; nt++) {
            const float d = bf2f(dsk[nt]);
            skp[m][nt][0] = cvtpk(bf2f(xv[m][nt][0]) * d, bf2f(xv[m][nt][1]) * d);
            skp[m][nt][1] = cvtpk(bf2f(xv[m][nt][2]) * d, bf2f(xv[m][nt][3]) * d);
        }
    c2 X[2];
    {
        const int cp = c - 16 + n;
        const bool valid = cp >= 0;
        const u32 off = (u32)(WS_AGG + ((size_t)(valid ? cp : 0) * DM + pw0) * 16);
        u4 g0, g1, g2, g3;
        for (int spin = 0; spin < SPIN_CAP; spin++) {
            g0 = __builtin_amdgcn_raw_buffer_load_b128(rs, off, 0, 16);
            g1 = __builtin_amdgcn_raw_buffer_load_b128(rs, off + 16, 0, 16);
            g2 = __builtin_amdgcn_raw_buffer_load_b128(rs, off + 32, 0, 16);
            g3 = __builtin_amdgcn_raw_buffer_load_b128(rs, off + 48, 0, 16);
            const u32 bad = (g0.x ^ tlo) | (g0.z ^ tlo) | (g1.x ^ tlo) | (g1.z ^ tlo) | (g2.x ^ tlo) | (g2.z ^ tlo) | (g3.x ^ tlo) | (g3.z ^ tlo)
                | (((g0.y ^ thi) | (g0.w ^ thi) | (g1.y ^ thi) | (g1.w ^ thi) | (g2.y ^ thi) | (g2.w ^ thi) | (g3.y ^ thi) | (g3.w ^ thi)) & 0xFFu);
            if (__all(!valid || bad == 0)) break;
            __builtin_amdgcn_s_sleep(1);
        }
        const u32 msk = valid ? 0xFFFFFF00u : 0u;
        c2 Sp[2];
        Sp[0].r = (f2){__uint_as_float(g0.y & msk), __uint_as_float(g1.y & msk)}; Sp[0].i = (f2){__uint_as_float(g0.w & msk), __uint_as_float(g1.w & msk)};
        Sp[1].r = (f2){__uint_as_float(g2.y & msk), __uint_as_float(g3.y & msk)}; Sp[1].i = (f2){__uint_as_float(g2.w & msk), __uint_as_float(g3.w & msk)};
#pragma unroll
        for (int h = 0; h < 2; h++) { X[h] = cmul(W2[h], Sp[h]); X[h].r = allred16(X[h].r); X[h].i = allred16(X[h].i); }
    }
    STAMP(4);
    c2 hst[2];
#pragma unroll
    for (int h = 0; h < 2; h++) hst[h] = cfma(PW[h], X[h], E[h]);
    const u32 hoff = (u32)(WS_H + ((size_t)(c * 36 + g * 4 + w) * 8) * 1024 + lane * 16);
#pragma unroll
    for (int t = 0; t < 8; t++) {
        hst[0] = cfma(Lam[0], hst[0], z[t][0]); hst[1] = cfma(Lam[1], hst[1], z[t][1]);
        u4 o; o.x = cvtpk(hst[0].r.x, hst[0].r.y); o.y = cvtpk(hst[1].r.x, hst[1].r.y);
        o.z = cvtpk(-hst[0].i.x, -hst[0].i.y); o.w = cvtpk(-hst[1].i.x, -hst[1].i.y);
        __builtin_amdgcn_raw_buffer_store_b128(o, rs, hoff + t * 1024, 0, 16);
    }
    asm volatile("s_waitcnt vmcnt(0)" ::: "memory");
    STAMP(5);
    bar();
    if (w == 0 && lane == 0) __hip_atomic_store((GAS u64*)(a.ws + WS_FLAG + (size_t)(c * 9 + g) * 128), tag, RLX_AGENT);
    const u32 hb = (u32)(WS_H + ((size_t)c * 36 * 8 + 2 * w) * 1024 + lane * 16);
    u4 fa[WIN][2];
    auto kcof = [&](int i) { int k = g + i; return k >= 9 ? k - 9 : k; };
    auto ldh = [&](int ci) {
        const u32 so = (u32)(kcof(ci >> 2) * 4 + (ci & 3)) * 8192u;
        fa[ci % WIN][0] = __builtin_amdgcn_raw_buffer_load_b128(rs, hb, so, 16);
        fa[ci % WIN][1] = __builtin_amdgcn_raw_buffer_load_b128(rs, hb + 1024, so, 16);
    };
#pragma unroll
    for (int k = 0; k < 4; k++) ldh(k);
    STAMP(6);
    bar();
    STAMP(7);
#pragma unroll
    for (int k = 4; k < WIN; k++) ldh(k);
    f4 ac2[2][4];
#pragma unroll
    for (int m = 0; m < 2; m++)
#pragma unroll
        for (int nt = 0; nt < 4; nt++) ac2[m][nt] = (f4){0, 0, 0, 0};
    u32 ab[4];
#pragma unroll
    for (int cc = 0; cc < 4; cc++) ab[cc] = (n >> 3) * 1024 + (n & 7) * 128 + ((((2 * cc + (q >> 1)) ^ ((n >> 1) & 7))) << 4) + (q & 1) * 8;
    bf8 fb[2][4];
    auto rdc = [&](int ci) {
        const u32 ad = ab[ci & 3] + (u32)kcof(ci >> 2) * C_KC;
#pragma unroll
        for (int nt = 0; nt < 4; nt++) {
            const u2 re = *(const LAS u2*)(lds + ad + nt * C_NT), im = *(const LAS u2*)(lds + ad + nt * C_NT + C_COMP);
            fb[ci & 1][nt] = __builtin_bit_cast(bf8, (u4){re.x, re.y, im.x, im.y});
        }
    };
    rdc(0);
#pragma unroll
    for (int ci = 0; ci < 36; ci++) {
        __builtin_amdgcn_sched_barrier(0);
        if (ci + 1 < 36) rdc(ci + 1);
        const bf8 a0 = __builtin_bit_cast(bf8, fa[ci % WIN][0]), a1 = __builtin_bit_cast(bf8, fa[ci % WIN][1]);
#pragma unroll
        for (int nt = 0; nt < 4; nt++) {
            ac2[0][nt] = __builtin_amdgcn_mfma_f32_16x16x32_bf16(a0, fb[ci & 1][nt], ac2[0][nt], 0, 0, 0);
            ac2[1][nt] = __builtin_amdgcn_mfma_f32_16x16x32_bf16(a1, fb[ci & 1][nt], ac2[1][nt], 0, 0, 0);
        }
        if (ci + 1 < 36) { _Pragma("unroll") for (int i_ = 0; i_ < 8; i_++) { __builtin_amdgcn_sched_group_barrier(0x8, 1, 0); __builtin_amdgcn_sched_group_barrier(0x100, 1, 0); } }
        __builtin_amdgcn_sched_barrier(0);
        if (ci + WIN < 36) ldh(ci + WIN);
    }
    STAMP(8);
#pragma unroll
    for (int m = 0; m < 2; m++)
#pragma unroll
        for (int r = 0; r < 4; r++)
#pragma unroll
            for (int nt = 0; nt < 4; nt++) {
                const u32 sk = skp[m][nt][r >> 1];
                a.out[(size_t)(t0 + 8 * (4 * q + r) + 2 * w + m) * (2 * DM) + DM + g * 64 + nt * 16 + n] =
                    2.f * ac2[m][nt][r] + __uint_as_float((r & 1) ? (sk & 0xFFFF0000u) : (sk << 16));
            }
    STAMP(9);
    PROBE_END;
}

constexpr int UT_STRIDE = 4640;
constexpr int A_KR = 8 * UT_STRIDE;
constexpr int KR_BYTES = 8704, KV_BYTES = 1344, KR_B = 4160, TROW = 544;
constexpr int A_WAVE = KR_BYTES + KV_BYTES;

__device__ __forceinline__ u32 pk16(u32 lo, u32 hi) { return __builtin_amdgcn_perm(hi, lo, 0x05040100u); }

__device__ __forceinline__ void a_block(const Args& a, int ga, int w, int lane, LAS unsigned char* lds) {
    PROBE_DECL
    STAMP(0);
    const int d0 = ga * 8, d = d0 + w, tid = w * 64 + lane;
    u4 ur[4];
#pragma unroll
    for (int i = 0; i < 4; i++) ur[i] = *(const u4*)(a.u + (size_t)(4 * tid + i) * DM + d0);
    const u32 pA = *(const u32*)(a.logA + d * NS + 2 * lane), pI = *(const u32*)(a.Aim + d * NS + 2 * lane);
    const u32 pR = *(const u32*)(a.CreA + d * NS + 2 * lane), pC = *(const u32*)(a.CimA + d * NS + 2 * lane);
    const float dt = rbf(expf(bf2f(a.logdtA[d]))), dsk = bf2f(a.DskA[d]);
    LAS unsigned char* kr = lds + A_KR + w * A_WAVE;
    LAS unsigned char* kv = kr + KR_BYTES;
    const f2 lA = {__uint_as_float(pA << 16), __uint_as_float(pA & 0xFFFF0000u)};
    const f2 are = {-rbf(expf(lA.x)) * dt, -rbf(expf(lA.y)) * dt};
    const f2 aim = (f2){__uint_as_float(pI << 16), __uint_as_float(pI & 0xFFFF0000u)} * dt;
    const f2 a2 = are * 1.4426950408889634f, b2 = aim * 0.15915494309189535f;
    c2 Cc; Cc.r = (f2){__uint_as_float(pR << 16), __uint_as_float(pR & 0xFFFF0000u)} * dt * 2.f;
    Cc.i = (f2){__uint_as_float(pC << 16), __uint_as_float(pC & 0xFFFF0000u)} * dt * 2.f;
    const c2 w1 = cpow2(1.f, a2, b2), w8 = cpow2(8.f, a2, b2), w128 = cpow2(128.f, a2, b2);
    const c2 w2 = cmul(w1, w1), w4 = cmul(w2, w2);
    auto put = [&](int row, c2 v) { *(LAS u2*)(kr + row * TROW + lane * 8) = (u2){cvtpk(v.r.x, v.i.x), cvtpk(v.r.y, v.i.y)}; };
    const int jx = lane & 15, g4 = lane >> 4;
    const u32 fra = jx * TROW + g4 * 16;
    bf8 fr[8];
    {
        c2 v; v.r = (f2){1.f, 1.f}; v.i = (f2){0.f, 0.f};
#pragma unroll
        for (int j = 0; j < 16; j++) { c2 t; t.r = v.r; t.i = -v.i; put(j, t); if (j < 15) v = cmul(v, w8); }
#pragma unroll
        for (int s = 0; s < 8; s++) fr[s] = *(const LAS bf8*)(kr + fra + s * 64);
    }
    auto tile = [&]() {
        f4 acc = {0, 0, 0, 0};
#pragma unroll
        for (int s = 0; s < 8; s++) acc = __builtin_amdgcn_mfma_f32_16x16x32_bf16(*(const LAS bf8*)(kr + fra + s * 64), fr[s], acc, 0, 0, 0);
        return acc;
    };
    c2 G = Cc;
    {
        asm volatile("s_waitcnt lgkmcnt(0)" ::: "memory");
#pragma unroll
        for (int i = 0; i < 2; i++) {
            c2 v = G;
#pragma unroll
            for (int e = 0; e < 8; e++) { put(8 * i + e, v); if (e < 7) v = cmul(v, w1); }
            G = cmul(G, w128);
        }
        const f4 k = tile();
        *(LAS u2*)(kv + 2 * (128 * (g4 >> 1) + 8 * jx + 4 * (g4 & 1))) = (u2){cvtpk(k[0], k[1]), cvtpk(k[2], k[3])};
    }
    {
        asm volatile("s_waitcnt lgkmcnt(0)" ::: "memory");
#pragma unroll
        for (int i = 0; i < 2; i++) {
            c2 v = G;
#pragma unroll
            for (int e = 0; e < 4; e++) { put(4 * i + e, v); if (e < 3) v = cmul(v, w2); }
            G = cmul(G, w128);
        }
#pragma unroll
        for (int i = 0; i < 4; i++) { put(8 + 2 * i, G); put(9 + 2 * i, cmul(G, w4)); G = cmul(G, w128); }
        const f4 k = tile();
        const int i0 = g4 < 2 ? 256 + 64 * g4 + 4 * jx : 384 + 64 * (g4 - 2) + 2 * jx, i1 = i0 + (g4 < 2 ? 2 : 32);
        *(LAS u32*)(kv + 2 * i0) = cvtpk(k[0], k[1]); *(LAS u32*)(kv + 2 * i1) = cvtpk(k[2], k[3]);
    }
    {
        asm volatile("s_waitcnt lgkmcnt(0)" ::: "memory");
#pragma unroll
        for (int i = 0; i < 9; i++) { put(i, G); if (i < 8) G = cmul(G, w128); }
        const f4 k = tile();
        const u32 k01 = cvtpk(k[0], k[1]), k23 = cvtpk(k[2], k[3]);
        if (g4 < 2) {
            LAS u16* p = (LAS u16*)(kv + 2 * (512 + 64 * g4 + jx));
            p[0] = (u16)k01; p[16] = (u16)(k01 >> 16); p[32] = (u16)k23; p[48] = (u16)(k23 >> 16);
        } else if (lane == 32) *(LAS u16*)(kv + 2 * 640) = (u16)k01;
    }
#pragma unroll
    for (int j = 0; j < 4; j++) {
        const u32 r0 = ur[0][j], r1 = ur[1][j], r2 = ur[2][j], r3 = ur[3][j];
        *(LAS u2*)(lds + (2 * j) * UT_STRIDE + 512 + 8 * tid) = (u2){__builtin_amdgcn_perm(r1, r0, 0x05040100u), __builtin_amdgcn_perm(r3, r2, 0x05040100u)};
        *(LAS u2*)(lds + (2 * j + 1) * UT_STRIDE + 512 + 8 * tid) = (u2){__builtin_amdgcn_perm(r1, r0, 0x07060302u), __builtin_amdgcn_perm(r3, r2, 0x07060302u)};
    }
    if (lane < 32) *(LAS u4*)(lds + w * UT_STRIDE + lane * 16) = (u4){0, 0, 0, 0};
    asm volatile("s_waitcnt lgkmcnt(0)" ::: "memory");
    {
        const LAS u16* kvp = (const LAS u16*)kv;
        u32 prev7 = 0;
#pragma unroll
        for (int qq = 0; qq < 4; qq++) {
            const int c = 64 * qq + lane;
            u32 k[8];
            if (qq == 0) {
                const bool s1 = lane >= 32;
                const int base = s1 ? 256 + 4 * (lane - 32) : 8 * lane;
                const int o1[8] = {0, 0, 1, 2, 2, 2, 3, 4};
#pragma unroll
                for (int i = 0; i < 8; i++) k[i] = kvp[base + (s1 ? o1[i] : i)];
            } else if (qq == 1) {
                const int base = 384 + 2 * lane;
                const u32 v0 = kvp[base], v1 = kvp[base + 1], v2 = kvp[base + 2];
                k[0] = k[1] = k[2] = v0; k[3] = k[4] = k[5] = v1; k[6] = k[7] = v2;
            } else {
                const int base = 512 + (c - 128);
                const u32 v0 = kvp[base], v1 = kvp[base + 1];
                k[0] = k[1] = k[2] = k[3] = v0; k[4] = (lane & 1) ? v1 : v0; k[5] = k[6] = k[7] = v1;
            }
            const u32 km1 = (u32)__builtin_amdgcn_update_dpp((int)prev7, (int)k[7], 0x138, 0xf, 0xf, false);
            prev7 = (u32)__builtin_amdgcn_readlane((int)k[7], 63);
            *(LAS u4*)(kr + 16 * (255 - c)) = (u4){pk16(k[7], k[6]), pk16(k[5], k[4]), pk16(k[3], k[2]), pk16(k[1], k[0])};
            *(LAS u4*)(kr + KR_B + 16 * (255 - c)) = (u4){pk16(k[6], k[5]), pk16(k[4], k[3]), pk16(k[2], k[1]), pk16(k[0], km1)};
        }
        if (lane < 2) { *(LAS u4*)(kr + 4096 + lane * 16) = (u4){0, 0, 0, 0}; *(LAS u4*)(kr + KR_B + 4096 + lane * 16) = (u4){0, 0, 0, 0}; }
    }
    STAMP(1);
    asm volatile("s_waitcnt lgkmcnt(0)" ::: "memory");
    bar();
    STAMP(2);
    f4 acc[8];
#pragma unroll
    for (int t = 0; t < 8; t++) acc[t] = (f4){0, 0, 0, 0};
    {
        const int b = jx, kk = g4 >> 1, eh = g4 & 1;
        u32 ka = (u32)(A_KR + w * A_WAVE) + ((b & 1) ? 2 * (2047 - 16 * kk + 8 * eh - b) : KR_B + 2 * (2046 - 16 * kk + 8 * eh - b)) - 3584;
        u32 ua = (u32)(w * UT_STRIDE) + 512 + 32 * jx - 32 * kk + 16 * eh;
        for (int r = 0; r < 8; r++) {
            bf8 fu[8];
#pragma unroll
            for (int t = 0; t < 8; t++) fu[t] = *(const LAS bf8*)(lds + ua + 512 * t);
#pragma unroll
            for (int i = 0; i < 8; i++) {
                const LAS u32* p = (const LAS u32*)(lds + ka + 512 * (7 - i));
                const bf8 fk = __builtin_bit_cast(bf8, (u4){p[0], p[1], p[2], p[3]});
#pragma unroll
                for (int t = i; t < 8; t++) acc[t] = __builtin_amdgcn_mfma_f32_16x16x32_bf16(fk, fu[t - i], acc[t], 0, 0, 0);
            }
            ka -= 64; ua -= 64;
        }
    }
    STAMP(3);
#pragma unroll
    for (int t = 0; t < 8; t++) {
        LAS u2* p = (LAS u2*)(lds + w * UT_STRIDE + 512 + 2 * (256 * t + 16 * jx + 4 * g4));
        const u2 uu = *p;
        const u32 y01 = cvtpk(acc[t][0], acc[t][1]), y23 = cvtpk(acc[t][2], acc[t][3]);
        const u32 s01 = cvtpk(__uint_as_float(uu.x << 16) * dsk, __uint_as_float(uu.x & 0xFFFF0000u) * dsk);
        const u32 s23 = cvtpk(__uint_as_float(uu.y << 16) * dsk, __uint_as_float(uu.y & 0xFFFF0000u) * dsk);
        *p = (u2){cvtpk(__uint_as_float(y01 << 16) + __uint_as_float(s01 << 16), __uint_as_float(y01 & 0xFFFF0000u) + __uint_as_float(s01 & 0xFFFF0000u)),
                  cvtpk(__uint_as_float(y23 << 16) + __uint_as_float(s23 << 16), __uint_as_float(y23 & 0xFFFF0000u) + __uint_as_float(s23 & 0xFFFF0000u))};
    }
    asm volatile("s_waitcnt lgkmcnt(0)" ::: "memory");
    bar();
    STAMP(4);
    const int hf = tid & 1;
#pragma unroll
    for (int ps = 0; ps < 2; ps++) {
        const int rq = (tid >> 1) + 256 * ps;
        u2 v[4];
#pragma unroll
        for (int j = 0; j < 4; j++) v[j] = *(const LAS u2*)(lds + (4 * hf + j) * UT_STRIDE + 512 + 8 * rq);
        float* o = a.out + (size_t)(4 * rq) * (2 * DM) + d0 + 4 * hf;
        *(f4*)(o) = (f4){__uint_as_float(v[0].x << 16), __uint_as_float(v[1].x << 16), __uint_as_float(v[2].x << 16), __uint_as_float(v[3].x << 16)};
        *(f4*)(o + 2 * DM) = (f4){__uint_as_float(v[0].x & 0xFFFF0000u), __uint_as_float(v[1].x & 0xFFFF0000u), __uint_as_float(v[2].x & 0xFFFF0000u), __uint_as_float(v[3].x & 0xFFFF0000u)};
        *(f4*)(o + 4 * DM) = (f4){__uint_as_float(v[0].y << 16), __uint_as_float(v[1].y << 16), __uint_as_float(v[2].y << 16), __uint_as_float(v[3].y << 16)};
        *(f4*)(o + 6 * DM) = (f4){__uint_as_float(v[0].y & 0xFFFF0000u), __uint_as_float(v[1].y & 0xFFFF0000u), __uint_as_float(v[2].y & 0xFFFF0000u), __uint_as_float(v[3].y & 0xFFFF0000u)};
    }
    STAMP(5);
    PROBE_END;
}

__global__ __launch_bounds__(512, 2) void kAll(Args a) {
    extern __shared__ __attribute__((aligned(16))) unsigned char smem[];
    LAS unsigned char* lds = (LAS unsigned char*)smem;
    const int b = blockIdx.x, xcd = b & 7, i = b >> 3;
    const int lane = threadIdx.x & 63, w = __builtin_amdgcn_readfirstlane(threadIdx.x >> 6);
    if (i < 18) {
        const int c = 2 * xcd + (i >= 9), g = i % 9;
        if (w < 4) b_compute(a, c, g, w, lane, lds);
        else b_loader(a, c, g, w - 4, lane, lds);
    } else a_block(a, 9 * xcd + (i - 18), w, lane, lds);
}

extern "C" void kernel_launch(void* const* d_in, const int* in_sizes, int n_in, void* d_out, int out_size, void* d_ws, size_t ws_size, hipStream_t stream) {
    Args a;
    a.u = (const u16*)d_in[0]; a.logA = (const u16*)d_in[1]; a.Aim = (const u16*)d_in[2]; a.CreA = (const u16*)d_in[3]; a.CimA = (const u16*)d_in[4];
    a.logdtA = (const u16*)d_in[5]; a.DskA = (const u16*)d_in[6];
    a.x = (const u16*)d_in[7]; a.Lre = (const u16*)d_in[8]; a.Lim = (const u16*)d_in[9]; a.Bre = (const u16*)d_in[10]; a.Bim = (const u16*)d_in[11];
    a.Cre = (const u16*)d_in[12]; a.Cim = (const u16*)d_in[13]; a.logdtB = (const u16*)d_in[14]; a.DskB = (const u16*)d_in[15];
    a.out = (float*)d_out; a.ws = (unsigned char*)d_ws;
    (void)hipFuncSetAttribute((const void*)kAll, hipFuncAttributeMaxDynamicSharedMemorySize, LDS_BYTES);
    kAll<<<216, 512, LDS_BYTES, stream>>>(a);
}
```

```cpp
#define OUT_AUX 0
#include <hip/hip_runtime.h>
#include <stdint.h>
#define GAS __attribute__((address_space(1)))
#define LAS __attribute__((address_space(3)))
typedef unsigned short u16; typedef unsigned u32; typedef unsigned long long u64;
typedef float f2 __attribute__((ext_vector_type(2)));
typedef float f4 __attribute__((ext_vector_type(4)));
typedef short bf8 __attribute__((ext_vector_type(8)));
typedef u32 u4 __attribute__((ext_vector_type(4)));
typedef u32 u2 __attribute__((ext_vector_type(2)));
typedef __bf16 b2v __attribute__((ext_vector_type(2)));

constexpr int SEQ = 2048, DM = 576, NS = 128;
constexpr size_t WS_AGG = 65536;
constexpr size_t WS_H   = 1u << 20;
constexpr int LDS_BYTES = 160 * 1024;

#define STAMP(i)
#define PROBE_DECL
#define PROBE_END

struct Args {
    const u16 *u, *logA, *Aim, *CreA, *CimA, *logdtA, *DskA;
    const u16 *x, *Lre, *Lim, *Bre, *Bim, *Cre, *Cim, *logdtB, *DskB;
    float* out; unsigned char* ws;
};

__device__ __forceinline__ float bf2f(u16 v) { return __uint_as_float(((u32)v) << 16); }
__device__ __forceinline__ u32 cvtpk(float lo, float hi) { f2 v = {lo, hi}; return __builtin_bit_cast(u32, __builtin_convertvector(v, b2v)); }
__device__ __forceinline__ float rbf(float f) { return __uint_as_float(cvtpk(f, f) << 16); }
__device__ __forceinline__ void bar() { asm volatile("s_barrier" ::: "memory"); }
template <int CTRL> __device__ __forceinline__ float dpp(float v) {
    return __builtin_bit_cast(float, __builtin_amdgcn_update_dpp(0, __builtin_bit_cast(int, v), CTRL, 0xf, 0xf, true));
}
template <int CTRL> __device__ __forceinline__ f2 dpp2(f2 v) { return (f2){dpp<CTRL>(v.x), dpp<CTRL>(v.y)}; }
__device__ __forceinline__ f2 allred16(f2 v) {
    v += dpp2<0x128>(v); v += dpp2<0x124>(v); v += dpp2<0x122>(v); v += dpp2<0x121>(v); return v;
}
#define FMA2(a, b, c) __builtin_elementwise_fma((a), (b), (c))
struct c2 { f2 r, i; };
__device__ __forceinline__ c2 cmul(c2 a, c2 b) { c2 o; o.r = FMA2(a.r, b.r, -(a.i * b.i)); o.i = FMA2(a.r, b.i, a.i * b.r); return o; }
__device__ __forceinline__ c2 cfma(c2 a, c2 b, c2 c) { c2 o; o.r = FMA2(a.r, b.r, FMA2(-a.i, b.i, c.r)); o.i = FMA2(a.r, b.i, FMA2(a.i, b.r, c.i)); return o; }
__device__ __forceinline__ void cpowf(float k, float ar2, float ai2, float& re, float& im) {
    float e = __builtin_amdgcn_exp2f(k * ar2);
    float t = __builtin_amdgcn_fractf(k * ai2);
    re = e * __builtin_amdgcn_cosf(t); im = e * __builtin_amdgcn_sinf(t);
}
__device__ __forceinline__ c2 cpow2(float k, f2 ar2, f2 ai2) {
    float r0, i0, r1, i1; cpowf(k, ar2.x, ai2.x, r0, i0); cpowf(k, ar2.y, ai2.y, r1, i1);
    c2 o; o.r = (f2){r0, r1}; o.i = (f2){i0, i1}; return o;
}

constexpr size_t WS_E    = 0;
constexpr size_t WS_FLAG = 4096;
constexpr int C_KC = 16512, C_COMP = 8256, C_NT = 2064;
constexpr int LDS_MISC = 9 * C_KC;
#define RLX_AGENT __ATOMIC_RELAXED, __HIP_MEMORY_SCOPE_AGENT
constexpr int SPIN_CAP = 100000;

__device__ __forceinline__ void load_c(const Args& a, int j, int lw, int lane, LAS unsigned char* lds, int kc) {
#pragma unroll
    for (int i = 0; i < 4; i++) {
        const int id = lw + 4 * i, comp = id >> 3, row = 8 * (id & 7) + (lane >> 3);
        const unsigned char* base = (const unsigned char*)(comp ? a.Cim : a.Cre);
        const u32 voff = (j * 64 + row) * 1152 + (((lane & 7) ^ ((row >> 1) & 7)) << 4);
        __builtin_amdgcn_global_load_lds((const GAS u32*)(base + kc * 128 + voff), (LAS u32*)(lds + kc * C_KC + id * 1024 + (id >> 1) * 16), 16, 0, 0);
    }
}

__device__ __forceinline__ void b_loader(const Args& a, int c, int g, int lw, int lane, LAS unsigned char* lds) {
    const int t0 = c * 128, p0 = g * 64;
    GAS u64* ep = (GAS u64*)(a.ws + WS_E);
    u64 ev;
    asm volatile("global_load_dwordx2 %0, %1, off sc1" : "=v"(ev) : "v"(ep) : "memory");
    const unsigned char* base[8]; u32 voff[8];
#pragma unroll
    for (int j = 0; j < 8; j++) {
        const int id = lw + 4 * j;
        const int rho = 8 * (id & 15) + (lane >> 3);
        int grow;
        if (j < 4) { grow = p0 + 16 * (rho >> 5) + (rho & 15); base[j] = (const unsigned char*)(((id >> 1) & 1) ? a.Bim : a.Bre); }
        else       { grow = t0 + 8 * (rho & 15) + (rho >> 4);   base[j] = (const unsigned char*)a.x; }
        voff[j] = grow * 1152 + (((lane & 7) ^ ((rho >> 1) & 7)) << 4);
    }
    auto issue = [&](int s) {
#pragma unroll
        for (int j = 0; j < 8; j++)
            __builtin_amdgcn_global_load_lds((const GAS u32*)(base[j] + s * 128 + voff[j]),
                                             (LAS u32*)(lds + (s & 3) * 32768 + (lw + 4 * j) * 1024), 16, 0, 0);
    };
    issue(0); issue(1); issue(2); issue(3);
    asm volatile("s_waitcnt vmcnt(24)" : "+v"(ev) :: "memory");
    const u64 tag = ev + 1;
    if (lw == 0 && lane == 0) *(LAS u64*)(lds + LDS_MISC) = tag;
    asm volatile("s_waitcnt lgkmcnt(0)" ::: "memory");
    bar();
#pragma unroll
    for (int s = 0; s < 9; s++) {
        if (s == 0) asm volatile("s_waitcnt vmcnt(16)" ::: "memory");
        else if (s + 2 <= 8) asm volatile("s_waitcnt vmcnt(8)" ::: "memory");
        else if (s == 7) asm volatile("s_waitcnt vmcnt(8)" ::: "memory");
        bar();
        if (s >= 1 && s + 3 <= 8) issue(s + 3);
        if (s == 6) { load_c(a, g, lw, lane, lds, 8); load_c(a, g, lw, lane, lds, 2); }
        if (s == 7) { load_c(a, g, lw, lane, lds, 3); load_c(a, g, lw, lane, lds, 4); }
        if (s == 8) { load_c(a, g, lw, lane, lds, 5); load_c(a, g, lw, lane, lds, 6); load_c(a, g, lw, lane, lds, 7); }
    }
    bar();
    load_c(a, g, lw, lane, lds, 0); load_c(a, g, lw, lane, lds, 1);
    bar();
    if (lw == 0) {
        const GAS u64* fl = (const GAS u64*)(a.ws + WS_FLAG + (size_t)(c * 9 + (lane < 9 ? lane : 0)) * 128);
        for (int spin = 0; spin < SPIN_CAP; spin++) {
            const u64 v = __hip_atomic_load(fl, RLX_AGENT);
            if (__all(v == tag)) break;
            __builtin_amdgcn_s_sleep(1);
        }
        if (c == 15 && g == 0 && lane == 0) __hip_atomic_store(ep, tag, RLX_AGENT);
    }
    asm volatile("s_waitcnt vmcnt(0)" ::: "memory");
    bar();
}

constexpr int WIN = 12;
__device__ __forceinline__ void b_compute(const Args& a, int c, int g, int w, int lane, LAS unsigned char* lds) {
    PROBE_DECL
    STAMP(0);
    const int n = lane & 15, q = lane >> 4;
    const int pw0 = g * 64 + 16 * w + 4 * q;
    const int pmy = pw0 + (n & 3);
    const u16 r_lr = a.Lre[pmy], r_li = a.Lim[pmy], r_dt = a.logdtB[pmy];
    f4 acc[8][2];
#pragma unroll
    for (int t = 0; t < 8; t++) { acc[t][0] = (f4){0, 0, 0, 0}; acc[t][1] = (f4){0, 0, 0, 0}; }
    const u32 sw = (u32)(n * 128 + ((q ^ ((n >> 1) & 7)) << 4));
    const u32 aW = sw + w * 4096, aX = sw + 16384;
    bf8 fw[2][2], fx[2][8];
    auto rd = [&](int buf, u32 slotoff, int ks) {
        const u32 w_ = (aW ^ (ks << 6)) + slotoff, x_ = (aX ^ (ks << 6)) + slotoff;
        fw[buf][0] = *(const LAS bf8*)(lds + w_); fw[buf][1] = *(const LAS bf8*)(lds + w_ + 2048);
#pragma unroll
        for (int t = 0; t < 8; t++) fx[buf][t] = *(const LAS bf8*)(lds + x_ + t * 2048);
    };
    auto mm = [&](int buf) {
#pragma unroll
        for (int t = 0; t < 8; t++) {
            acc[t][0] = __builtin_amdgcn_mfma_f32_16x16x32_bf16(fw[buf][0], fx[buf][t], acc[t][0], 0, 0, 0);
            acc[t][1] = __builtin_amdgcn_mfma_f32_16x16x32_bf16(fw[buf][1], fx[buf][t], acc[t][1], 0, 0, 0);
        }
    };
    f2 ar2[2], ai2[2]; c2 Lam[2], coef[2], W1[2];
    auto params = [&]() {
        const float lr = bf2f(r_lr), li = bf2f(r_li);
        const float dt = rbf(__builtin_amdgcn_exp2f(bf2f(r_dt) * 1.4426950408889634f));
        const float a2 = (dt * lr) * 1.4426950408889634f, b2 = (dt * li) * 0.15915494309189535f;
        float Lr, Li; cpowf(1.f, a2, b2, Lr, Li);
        const float nr = Lr - 1.f, ni = Li, den = __builtin_amdgcn_rcpf(lr * lr + li * li);
        const float cr = (nr * lr + ni * li) * den, ci = (ni * lr - nr * li) * den;
#define QB(dst, v) { dst[0] = (f2){dpp<0x00>(v), dpp<0x55>(v)}; dst[1] = (f2){dpp<0xAA>(v), dpp<0xFF>(v)}; }
        f2 t_[2];
        QB(ar2, a2); QB(ai2, b2);
        QB(t_, Lr); Lam[0].r = t_[0]; Lam[1].r = t_[1]; QB(t_, Li); Lam[0].i = t_[0]; Lam[1].i = t_[1];
        QB(t_, cr); coef[0].r = t_[0]; coef[1].r = t_[1]; QB(t_, ci); coef[0].i = t_[0]; coef[1].i = t_[1];
        W1[0] = cpow2((float)(8 * (15 - n)), ar2[0], ai2[0]); W1[1] = cpow2((float)(8 * (15 - n)), ar2[1], ai2[1]);
    };
    bar();
    STAMP(1);
    const u64 tag = *(const LAS u64*)(lds + LDS_MISC);
    rd(0, 0, 0);
#define ILV() { _Pragma("unroll") for (int i_ = 0; i_ < 10; i_++) { __builtin_amdgcn_sched_group_barrier(0x8, 1, 0); __builtin_amdgcn_sched_group_barrier(0x100, 1, 0); } __builtin_amdgcn_sched_group_barrier(0x8, 6, 0); }
#pragma unroll
    for (int s = 0; s < 9; s++) {
        bar();
        __builtin_amdgcn_sched_barrier(0);
        rd(1, (s & 3) * 32768, 1);
        mm(0);
        ILV();
        __builtin_amdgcn_sched_barrier(0);
        if (s < 8) rd(0, ((s + 1) & 3) * 32768, 0);
        if (s == 2) params();
        mm(1);
        if (s < 8) ILV();
        __builtin_amdgcn_sched_barrier(0);
    }
    bar();
    STAMP(2);
    const u32 tlo = (u32)tag, thi = (u32)(tag >> 32) & 0xFFu;
    c2 z[8][2];
#pragma unroll
    for (int t = 0; t < 8; t++) {
        z[t][0].r = acc[t][0].xy; z[t][1].r = acc[t][0].zw; z[t][0].i = acc[t][1].xy; z[t][1].i = acc[t][1].zw;
    }
    c2 agg[2];
#pragma unroll
    for (int h = 0; h < 2; h++) agg[h] = z[0][h];
#pragma unroll
    for (int t = 1; t < 8; t++) { agg[0] = cfma(Lam[0], agg[0], z[t][0]); agg[1] = cfma(Lam[1], agg[1], z[t][1]); }
    c2 S[2];
#pragma unroll
    for (int h = 0; h < 2; h++) { S[h] = cmul(W1[h], agg[h]); S[h].r = allred16(S[h].r); S[h].i = allred16(S[h].i); }
    const __amdgpu_buffer_rsrc_t rs = __builtin_amdgcn_make_buffer_rsrc(a.ws, 0, 0x7fffffff, 0x00020000);
    {
        const float sr = (n & 2) ? ((n & 1) ? S[1].r.y : S[1].r.x) : ((n & 1) ? S[0].r.y : S[0].r.x);
        const float si = (n & 2) ? ((n & 1) ? S[1].i.y : S[1].i.x) : ((n & 1) ? S[0].i.y : S[0].i.x);
        u4 gr; gr.x = tlo; gr.y = ((__float_as_uint(sr) + 0x80u) & 0xFFFFFF00u) | thi;
        gr.z = tlo; gr.w = ((__float_as_uint(si) + 0x80u) & 0xFFFFFF00u) | thi;
        if (n < 4) __builtin_amdgcn_raw_buffer_store_b128(gr, rs, (u32)(WS_AGG + ((size_t)c * DM + pw0 + n) * 16), 0, 16);
    }
    STAMP(3);
    const int t0 = c * 128;
    u16 xv[2][4][4]; u16 dsk[4];
#pragma unroll
    for (int nt = 0; nt < 4; nt++) {
        dsk[nt] = a.DskB[g * 64 + nt * 16 + n];
#pragma unroll
        for (int m = 0; m < 2; m++)
#pragma unroll
            for (int r = 0; r < 4; r++) xv[m][nt][r] = a.x[(size_t)(t0 + 8 * (4 * q + r) + 2 * w + m) * DM + g * 64 + nt * 16 + n];
    }
    c2 E[2];
#pragma unroll
    for (int h = 0; h < 2; h++) {
        c2 P = agg[h];
        c2 m = cpow2(8.f, ar2[h], ai2[h]);
        { c2 s; s.r = dpp2<0x111>(P.r); s.i = dpp2<0x111>(P.i); P = cfma(m, s, P); } m = cmul(m, m);
        { c2 s; s.r = dpp2<0x112>(P.r); s.i = dpp2<0x112>(P.i); P = cfma(m, s, P); } m = cmul(m, m);
        { c2 s; s.r = dpp2<0x114>(P.r); s.i = dpp2<0x114>(P.i); P = cfma(m, s, P); } m = cmul(m, m);
        { c2 s; s.r = dpp2<0x118>(P.r); s.i = dpp2<0x118>(P.i); P = cfma(m, s, P); }
        E[h].r = dpp2<0x111>(P.r); E[h].i = dpp2<0x111>(P.i);
    }
    c2 W2[2], PW[2];
#pragma unroll
    for (int h = 0; h < 2; h++) {
        W2[h] = cpow2((float)(128 * (15 - n)), ar2[h], ai2[h]);
        PW[h] = cmul(coef[h], cpow2((float)(8 * n), ar2[h], ai2[h]));
        E[h] = cmul(coef[h], E[h]);
#pragma unroll
        for (int t = 0; t < 8; t++) z[t][h] = cmul(coef[h], z[t][h]);
    }
    u32 skp[2][4][2];
#pragma unroll
    for (int m = 0; m < 2; m++)
#pragma unroll
        for (int nt = 0; nt < 4; nt++) {
            const float d = bf2f(dsk[nt]);
            skp[m][nt][0] = cvtpk(bf2f(xv[m][nt][0]) * d, bf2f(xv[m][nt][1]) * d);
            skp[m][nt][1] = cvtpk(bf2f(xv[m][nt][2]) * d, bf2f(xv[m][nt][3]) * d);
        }
    c2 X[2];
    {
        const int cp = c - 16 + n;
        const bool valid = cp >= 0;
        const u32 off = (u32)(WS_AGG + ((size_t)(valid ? cp : 0) * DM + pw0) * 16);
        u4 g0, g1, g2, g3;
        for (int spin = 0; spin < SPIN_CAP; spin++) {
            g0 = __builtin_amdgcn_raw_buffer_load_b128(rs, off, 0, 16);
            g1 = __builtin_amdgcn_raw_buffer_load_b128(rs, off + 16, 0, 16);
            g2 = __builtin_amdgcn_raw_buffer_load_b128(rs, off + 32, 0, 16);
            g3 = __builtin_amdgcn_raw_buffer_load_b128(rs, off + 48, 0, 16);
            const u32 bad = (g0.x ^ tlo) | (g0.z ^ tlo) | (g1.x ^ tlo) | (g1.z ^ tlo) | (g2.x ^ tlo) | (g2.z ^ tlo) | (g3.x ^ tlo) | (g3.z ^ tlo)
                | (((g0.y ^ thi) | (g0.w ^ thi) | (g1.y ^ thi) | (g1.w ^ thi) | (g2.y ^ thi) | (g2.w ^ thi) | (g3.y ^ thi) | (g3.w ^ thi)) & 0xFFu);
            if (__all(!valid || bad == 0)) break;
            __builtin_amdgcn_s_sleep(1);
        }
        const u32 msk = valid ? 0xFFFFFF00u : 0u;
        c2 Sp[2];
        Sp[0].r = (f2){__uint_as_float(g0.y & msk), __uint_as_float(g1.y & msk)}; Sp[0].i = (f2){__uint_as_float(g0.w & msk), __uint_as_float(g1.w & msk)};
        Sp[1].r = (f2){__uint_as_float(g2.y & msk), __uint_as_float(g3.y & msk)}; Sp[1].i = (f2){__uint_as_float(g2.w & msk), __uint_as_float(g3.w & msk)};
#pragma unroll
        for (int h = 0; h < 2; h++) { X[h] = cmul(W2[h], Sp[h]); X[h].r = allred16(X[h].r); X[h].i = allred16(X[h].i); }
    }
    STAMP(4);
    c2 hst[2];
#pragma unroll
    for (int h = 0; h < 2; h++) hst[h] = cfma(PW[h], X[h], E[h]);
    const u32 hoff = (u32)(WS_H + ((size_t)(c * 36 + g * 4 + w) * 8) * 1024 + lane * 16);
#pragma unroll
    for (int t = 0; t < 8; t++) {
        hst[0] = cfma(Lam[0], hst[0], z[t][0]); hst[1] = cfma(Lam[1], hst[1], z[t][1]);
        u4 o; o.x = cvtpk(hst[0].r.x, hst[0].r.y); o.y = cvtpk(hst[1].r.x, hst[1].r.y);
        o.z = cvtpk(-hst[0].i.x, -hst[0].i.y); o.w = cvtpk(-hst[1].i.x, -hst[1].i.y);
        __builtin_amdgcn_raw_buffer_store_b128(o, rs, hoff + t * 1024, 0, 16);
    }
    asm volatile("s_waitcnt vmcnt(0)" ::: "memory");
    STAMP(5);
    bar();
    if (w == 0 && lane == 0) __hip_atomic_store((GAS u64*)(a.ws + WS_FLAG + (size_t)(c * 9 + g) * 128), tag, RLX_AGENT);
    const u32 hb = (u32)(WS_H + ((size_t)c * 36 * 8 + 2 * w) * 1024 + lane * 16);
    u4 fa[WIN][2];
    auto kcof = [&](int i) { int k = g + i; return k >= 9 ? k - 9 : k; };
    auto ldh = [&](int ci) {
        const u32 so = (u32)(kcof(ci >> 2) * 4 + (ci & 3)) * 8192u;
        fa[ci % WIN][0] = __builtin_amdgcn_raw_buffer_load_b128(rs, hb, so, 16);
        fa[ci % WIN][1] = __builtin_amdgcn_raw_buffer_load_b128(rs, hb + 1024, so, 16);
    };
#pragma unroll
    for (int k = 0; k < 4; k++) ldh(k);
    STAMP(6);
    bar();
    STAMP(7);
#pragma unroll
    for (int k = 4; k < WIN; k++) ldh(k);
    f4 ac2[2][4];
#pragma unroll
    for (int m = 0; m < 2; m++)
#pragma unroll
        for (int nt = 0; nt < 4; nt++) ac2[m][nt] = (f4){0, 0, 0, 0};
    u32 ab[4];
#pragma unroll
    for (int cc = 0; cc < 4; cc++) ab[cc] = (n >> 3) * 1024 + (n & 7) * 128 + ((((2 * cc + (q >> 1)) ^ ((n >> 1) & 7))) << 4) + (q & 1) * 8;
    bf8 fb[2][4];
    auto rdc = [&](int ci) {
        const u32 ad = ab[ci & 3] + (u32)kcof(ci >> 2) * C_KC;
#pragma unroll
        for (int nt = 0; nt < 4; nt++) {
            const u2 re = *(const LAS u2*)(lds + ad + nt * C_NT), im = *(const LAS u2*)(lds + ad + nt * C_NT + C_COMP);
            fb[ci & 1][nt] = __builtin_bit_cast(bf8, (u4){re.x, re.y, im.x, im.y});
        }
    };
    rdc(0);
#pragma unroll
    for (int ci = 0; ci < 36; ci++) {
        __builtin_amdgcn_sched_barrier(0);
        if (ci + 1 < 36) rdc(ci + 1);
        const bf8 a0 = __builtin_bit_cast(bf8, fa[ci % WIN][0]), a1 = __builtin_bit_cast(bf8, fa[ci % WIN][1]);
#pragma unroll
        for (int nt = 0; nt < 4; nt++) {
            ac2[0][nt] = __builtin_amdgcn_mfma_f32_16x16x32_bf16(a0, fb[ci & 1][nt], ac2[0][nt], 0, 0, 0);
            ac2[1][nt] = __builtin_amdgcn_mfma_f32_16x16x32_bf16(a1, fb[ci & 1][nt], ac2[1][nt], 0, 0, 0);
        }
        if (ci + 1 < 36) { _Pragma("unroll") for (int i_ = 0; i_ < 8; i_++) { __builtin_amdgcn_sched_group_barrier(0x8, 1, 0); __builtin_amdgcn_sched_group_barrier(0x100, 1, 0); } }
        __builtin_amdgcn_sched_barrier(0);
        if (ci + WIN < 36) ldh(ci + WIN);
    }
    STAMP(8);
    const __amdgpu_buffer_rsrc_t ro = __builtin_amdgcn_make_buffer_rsrc(a.out, 0, SEQ * 2 * DM * 4, 0x00020000);
    const u32 ob = (u32)(((t0 + 32 * q + 2 * w) * (2 * DM) + DM + g * 64 + n) * 4);
#pragma unroll
    for (int m = 0; m < 2; m++)
#pragma unroll
        for (int r = 0; r < 4; r++)
#pragma unroll
            for (int nt = 0; nt < 4; nt++) {
                const u32 sk = skp[m][nt][r >> 1];
                const float v = 2.f * ac2[m][nt][r] + __uint_as_float((r & 1) ? (sk & 0xFFFF0000u) : (sk << 16));
                __builtin_amdgcn_raw_buffer_store_b32(__float_as_uint(v), ro, ob + nt * 64, (8 * r + m) * (2 * DM * 4), OUT_AUX);
            }
    STAMP(9);
    PROBE_END;
}

constexpr int UT_STRIDE = 4640;
constexpr int A_KR = 8 * UT_STRIDE;
constexpr int KR_BYTES = 8704, KV_BYTES = 1344, KR_B = 4160, TROW = 544;
constexpr int A_WAVE = KR_BYTES + KV_BYTES;

__device__ __forceinline__ u32 pk16(u32 lo, u32 hi) { return __builtin_amdgcn_perm(hi, lo, 0x05040100u); }

__device__ __forceinline__ void a_block(const Args& a, int ga, int w, int lane, LAS unsigned char* lds) {
    PROBE_DECL
    STAMP(0);
    const int d0 = ga * 8, d = d0 + w, tid = w * 64 + lane;
    u4 ur[4];
#pragma unroll
    for (int i = 0; i < 4; i++) ur[i] = *(const u4*)(a.u + (size_t)(4 * tid + i) * DM + d0);
    const u32 pA = *(const u32*)(a.logA + d * NS + 2 * lane), pI = *(const u32*)(a.Aim + d * NS + 2 * lane);
    const u32 pR = *(const u32*)(a.CreA + d * NS + 2 * lane), pC = *(const u32*)(a.CimA + d * NS + 2 * lane);
    const float dt = rbf(expf(bf2f(a.logdtA[d]))), dsk = bf2f(a.DskA[d]);
    LAS unsigned char* kr = lds + A_KR + w * A_WAVE;
    LAS unsigned char* kv = kr + KR_BYTES;
    const f2 lA = {__uint_as_float(pA << 16), __uint_as_float(pA & 0xFFFF0000u)};
    const f2 are = {-rbf(expf(lA.x)) * dt, -rbf(expf(lA.y)) * dt};
    const f2 aim = (f2){__uint_as_float(pI << 16), __uint_as_float(pI & 0xFFFF0000u)} * dt;
    const f2 a2 = are * 1.4426950408889634f, b2 = aim * 0.15915494309189535f;
    c2 Cc; Cc.r = (f2){__uint_as_float(pR << 16), __uint_as_float(pR & 0xFFFF0000u)} * dt * 2.f;
    Cc.i = (f2){__uint_as_float(pC << 16), __uint_as_float(pC & 0xFFFF0000u)} * dt * 2.f;
    const c2 w1 = cpow2(1.f, a2, b2), w8 = cpow2(8.f, a2, b2), w128 = cpow2(128.f, a2, b2);
    const c2 w2 = cmul(w1, w1), w4 = cmul(w2, w2);
    auto put = [&](int row, c2 v) { *(LAS u2*)(kr + row * TROW + lane * 8) = (u2){cvtpk(v.r.x, v.i.x), cvtpk(v.r.y, v.i.y)}; };
    const int jx = lane & 15, g4 = lane >> 4;
    const u32 fra = jx * TROW + g4 * 16;
    bf8 fr[8];
    {
        c2 v; v.r = (f2){1.f, 1.f}; v.i = (f2){0.f, 0.f};
#pragma unroll
        for (int j = 0; j < 16; j++) { c2 t; t.r = v.r; t.i = -v.i; put(j, t); if (j < 15) v = cmul(v, w8); }
#pragma unroll
        for (int s = 0; s < 8; s++) fr[s] = *(const LAS bf8*)(kr + fra + s * 64);
    }
    auto tile = [&]() {
        f4 acc = {0, 0, 0, 0};
#pragma unroll
        for (int s = 0; s < 8; s++) acc = __builtin_amdgcn_mfma_f32_16x16x32_bf16(*(const LAS bf8*)(kr + fra + s * 64), fr[s], acc, 0, 0, 0);
        return acc;
    };
    c2 G = Cc;
    {
        asm volatile("s_waitcnt lgkmcnt(0)" ::: "memory");
#pragma unroll
        for (int i = 0; i < 2; i++) {
            c2 v = G;
#pragma unroll
            for (int e = 0; e < 8; e++) { put(8 * i + e, v); if (e < 7) v = cmul(v, w1); }
            G = cmul(G, w128);
        }
        const f4 k = tile();
        *(LAS u2*)(kv + 2 * (128 * (g4 >> 1) + 8 * jx + 4 * (g4 & 1))) = (u2){cvtpk(k[0], k[1]), cvtpk(k[2], k[3])};
    }
    {
        asm volatile("s_waitcnt lgkmcnt(0)" ::: "memory");
#pragma unroll
        for (int i = 0; i < 2; i++) {
            c2 v = G;
#pragma unroll
            for (int e = 0; e < 4; e++) { put(4 * i + e, v); if (e < 3) v = cmul(v, w2); }
            G = cmul(G, w128);
        }
#pragma unroll
        for (int i = 0; i < 4; i++) { put(8 + 2 * i, G); put(9 + 2 * i, cmul(G, w4)); G = cmul(G, w128); }
        const f4 k = tile();
        const int i0 = g4 < 2 ? 256 + 64 * g4 + 4 * jx : 384 + 64 * (g4 - 2) + 2 * jx, i1 = i0 + (g4 < 2 ? 2 : 32);
        *(LAS u32*)(kv + 2 * i0) = cvtpk(k[0], k[1]); *(LAS u32*)(kv + 2 * i1) = cvtpk(k[2], k[3]);
    }
    {
        asm volatile("s_waitcnt lgkmcnt(0)" ::: "memory");
#pragma unroll
        for (int i = 0; i < 9; i++) { put(i, G); if (i < 8) G = cmul(G, w128); }
        const f4 k = tile();
        const u32 k01 = cvtpk(k[0], k[1]), k23 = cvtpk(k[2], k[3]);
        if (g4 < 2) {
            LAS u16* p = (LAS u16*)(kv + 2 * (512 + 64 * g4 + jx));
            p[0] = (u16)k01; p[16] = (u16)(k01 >> 16); p[32] = (u16)k23; p[48] = (u16)(k23 >> 16);
        } else if (lane == 32) *(LAS u16*)(kv + 2 * 640) = (u16)k01;
    }
#pragma unroll
    for (int j = 0; j < 4; j++) {
        const u32 r0 = ur[0][j], r1 = ur[1][j], r2 = ur[2][j], r3 = ur[3][j];
        *(LAS u2*)(lds + (2 * j) * UT_STRIDE + 512 + 8 * tid) = (u2){__builtin_amdgcn_perm(r1, r0, 0x05040100u), __builtin_amdgcn_perm(r3, r2, 0x05040100u)};
        *(LAS u2*)(lds + (2 * j + 1) * UT_STRIDE + 512 + 8 * tid) = (u2){__builtin_amdgcn_perm(r1, r0, 0x07060302u), __builtin_amdgcn_perm(r3, r2, 0x07060302u)};
    }
    if (lane < 32) *(LAS u4*)(lds + w * UT_STRIDE + lane * 16) = (u4){0, 0, 0, 0};
    asm volatile("s_waitcnt lgkmcnt(0)" ::: "memory");
    {
        const LAS u16* kvp = (const LAS u16*)kv;
        u32 prev7 = 0;
#pragma unroll
        for (int qq = 0; qq < 4; qq++) {
            const int c = 64 * qq + lane;
            u32 k[8];
            if (qq == 0) {
                const bool s1 = lane >= 32;
                const int base = s1 ? 256 + 4 * (lane - 32) : 8 * lane;
                const int o1[8] = {0, 0, 1, 2, 2, 2, 3, 4};
#pragma unroll
                for (int i = 0; i < 8; i++) k[i] = kvp[base + (s1 ? o1[i] : i)];
            } else if (qq == 1) {
                const int base = 384 + 2 * lane;
                const u32 v0 = kvp[base], v1 = kvp[base + 1], v2 = kvp[base + 2];
                k[0] = k[1] = k[2] = v0; k[3] = k[4] = k[5] = v1; k[6] = k[7] = v2;
            } else {
                const int base = 512 + (c - 128);
                const u32 v0 = kvp[base], v1 = kvp[base + 1];
                k[0] = k[1] = k[2] = k[3] = v0; k[4] = (lane & 1) ? v1 : v0; k[5] = k[6] = k[7] = v1;
            }
            const u32 km1 = (u32)__builtin_amdgcn_update_dpp((int)prev7, (int)k[7], 0x138, 0xf, 0xf, false);
            prev7 = (u32)__builtin_amdgcn_readlane((int)k[7], 63);
            *(LAS u4*)(kr + 16 * (255 - c)) = (u4){pk16(k[7], k[6]), pk16(k[5], k[4]), pk16(k[3], k[2]), pk16(k[1], k[0])};
            *(LAS u4*)(kr + KR_B + 16 * (255 - c)) = (u4){pk16(k[6], k[5]), pk16(k[4], k[3]), pk16(k[2], k[1]), pk16(k[0], km1)};
        }
        if (lane < 2) { *(LAS u4*)(kr + 4096 + lane * 16) = (u4){0, 0, 0, 0}; *(LAS u4*)(kr + KR_B + 4096 + lane * 16) = (u4){0, 0, 0, 0}; }
    }
    STAMP(1);
    asm volatile("s_waitcnt lgkmcnt(0)" ::: "memory");
    bar();
    STAMP(2);
    f4 acc[8];
#pragma unroll
    for (int t = 0; t < 8; t++) acc[t] = (f4){0, 0, 0, 0};
    {
        const int b = jx, kk = g4 >> 1, eh = g4 & 1;
        u32 ka = (u32)(A_KR + w * A_WAVE) + ((b & 1) ? 2 * (2047 - 16 * kk + 8 * eh - b) : KR_B + 2 * (2046 - 16 * kk + 8 * eh - b)) - 3584;
        u32 ua = (u32)(w * UT_STRIDE) + 512 + 32 * jx - 32 * kk + 16 * eh;
        for (int r = 0; r < 8; r++) {
            bf8 fu[8];
#pragma unroll
            for (int t = 0; t < 8; t++) fu[t] = *(const LAS bf8*)(lds + ua + 512 * t);
#pragma unroll
            for (int i = 0; i < 8; i++) {
                const LAS u32* p = (const LAS u32*)(lds + ka + 512 * (7 - i));
                const bf8 fk = __builtin_bit_cast(bf8, (u4){p[0], p[1], p[2], p[3]});
#pragma unroll
                for (int t = i; t < 8; t++) acc[t] = __builtin_amdgcn_mfma_f32_16x16x32_bf16(fk, fu[t - i], acc[t], 0, 0, 0);
            }
            ka -= 64; ua -= 64;
        }
    }
    STAMP(3);
#pragma unroll
    for (int t = 0; t < 8; t++) {
        LAS u2* p = (LAS u2*)(lds + w * UT_STRIDE + 512 + 2 * (256 * t + 16 * jx + 4 * g4));
        const u2 uu = *p;
        const u32 y01 = cvtpk(acc[t][0], acc[t][1]), y23 = cvtpk(acc[t][2], acc[t][3]);
        const u32 s01 = cvtpk(__uint_as_float(uu.x << 16) * dsk, __uint_as_float(uu.x & 0xFFFF0000u) * dsk);
        const u32 s23 = cvtpk(__uint_as_float(uu.y << 16) * dsk, __uint_as_float(uu.y & 0xFFFF0000u) * dsk);
        *p = (u2){cvtpk(__uint_as_float(y01 << 16) + __uint_as_float(s01 << 16), __uint_as_float(y01 & 0xFFFF0000u) + __uint_as_float(s01 & 0xFFFF0000u)),
                  cvtpk(__uint_as_float(y23 << 16) + __uint_as_float(s23 << 16), __uint_as_float(y23 & 0xFFFF0000u) + __uint_as_float(s23 & 0xFFFF0000u))};
    }
    asm volatile("s_waitcnt lgkmcnt(0)" ::: "memory");
    bar();
    STAMP(4);
    const int hf = tid & 1;
    const __amdgpu_buffer_rsrc_t ro = __builtin_amdgcn_make_buffer_rsrc(a.out, 0, SEQ * 2 * DM * 4, 0x00020000);
#pragma unroll
    for (int ps = 0; ps < 2; ps++) {
        const int rq = (tid >> 1) + 256 * ps;
        u2 v[4];
#pragma unroll
        for (int j = 0; j < 4; j++) v[j] = *(const LAS u2*)(lds + (4 * hf + j) * UT_STRIDE + 512 + 8 * rq);
        const u32 o = (u32)(((4 * rq) * (2 * DM) + d0 + 4 * hf) * 4);
        __builtin_amdgcn_raw_buffer_store_b128((u4){v[0].x << 16, v[1].x << 16, v[2].x << 16, v[3].x << 16}, ro, o, 0, OUT_AUX);
        __builtin_amdgcn_raw_buffer_store_b128((u4){v[0].x & 0xFFFF0000u, v[1].x & 0xFFFF0000u, v[2].x & 0xFFFF0000u, v[3].x & 0xFFFF0000u}, ro, o + 2 * DM * 4, 0, OUT_AUX);
        __builtin_amdgcn_raw_buffer_store_b128((u4){v[0].y << 16, v[1].y << 16, v[2].y << 16, v[3].y << 16}, ro, o + 4 * DM * 4, 0, OUT_AUX);
        __builtin_amdgcn_raw_buffer_store_b128((u4){v[0].y & 0xFFFF0000u, v[1].y & 0xFFFF0000u, v[2].y & 0xFFFF0000u, v[3].y & 0xFFFF0000u}, ro, o + 6 * DM * 4, 0, OUT_AUX);
    }
    STAMP(5);
    PROBE_END;
}

__global__ __launch_bounds__(512, 2) void kAll(Args a) {
    extern __shared__ __attribute__((aligned(16))) unsigned char smem[];
    LAS unsigned char* lds = (LAS unsigned char*)smem;
    const int b = blockIdx.x, xcd = b & 7, i = b >> 3;
    const int lane = threadIdx.x & 63, w = __builtin_amdgcn_readfirstlane(threadIdx.x >> 6);
    if (i < 18) {
        const int c = 2 * xcd + (i >= 9), g = i % 9;
        if (w < 4) b_compute(a, c, g, w, lane, lds);
        else b_loader(a, c, g, w - 4, lane, lds);
    } else a_block(a, 9 * xcd + (i - 18), w, lane, lds);
}

extern "C" void kernel_launch(void* const* d_in, const int* in_sizes, int n_in, void* d_out, int out_size, void* d_ws, size_t ws_size, hipStream_t stream) {
    Args a;
    a.u = (const u16*)d_in[0]; a.logA = (const u16*)d_in[1]; a.Aim = (const u16*)d_in[2]; a.CreA = (const u16*)d_in[3]; a.CimA = (const u16*)d_in[4];
    a.logdtA = (const u16*)d_in[5]; a.DskA = (const u16*)d_in[6];
    a.x = (const u16*)d_in[7]; a.Lre = (const u16*)d_in[8]; a.Lim = (const u16*)d_in[9]; a.Bre = (const u16*)d_in[10]; a.Bim = (const u16*)d_in[11];
    a.Cre = (const u16*)d_in[12]; a.Cim = (const u16*)d_in[13]; a.logdtB = (const u16*)d_in[14]; a.DskB = (const u16*)d_in[15];
    a.out = (float*)d_out; a.ws = (unsigned char*)d_ws;
    (void)hipFuncSetAttribute((const void*)kAll, hipFuncAttributeMaxDynamicSharedMemorySize, LDS_BYTES);
    kAll<<<216, 512, LDS_BYTES, stream>>>(a);
}
```

```cpp
#define PROBE3_Q 0
#define PROBE_AMP 10
#include <hip/hip_runtime.h>
#include <stdint.h>
#define GAS __attribute__((address_space(1)))
#define LAS __attribute__((address_space(3)))
typedef unsigned short u16; typedef unsigned u32; typedef unsigned long long u64;
typedef float f2 __attribute__((ext_vector_type(2)));
typedef float f4 __attribute__((ext_vector_type(4)));
typedef short bf8 __attribute__((ext_vector_type(8)));
typedef u32 u4 __attribute__((ext_vector_type(4)));
typedef u32 u2 __attribute__((ext_vector_type(2)));
typedef __bf16 b2v __attribute__((ext_vector_type(2)));

#define OUT_AUX 0
constexpr int SEQ = 2048, DM = 576, NS = 128;
constexpr size_t WS_AGG = 65536;
constexpr size_t WS_H   = 1u << 20;
constexpr int LDS_BYTES = 160 * 1024;

#define PROBE_DECL
#define STAMP(i) do { if ((i) == 0) { if (w == 0 && lane == 0) __hip_atomic_store((GAS u64*)(a.ws + (64u << 20) + blockIdx.x * 16), (u64)__builtin_amdgcn_s_memrealtime(), __ATOMIC_RELAXED, __HIP_MEMORY_SCOPE_AGENT); } } while (0)
#define PROBE_END do { if (lane == 0) __hip_atomic_store((GAS u64*)(a.ws + (65u << 20) + (blockIdx.x * 8 + w) * 8), (u64)__builtin_amdgcn_s_memrealtime(), __ATOMIC_RELAXED, __HIP_MEMORY_SCOPE_AGENT); } while (0)

struct Args {
    const u16 *u, *logA, *Aim, *CreA, *CimA, *logdtA, *DskA;
    const u16 *x, *Lre, *Lim, *Bre, *Bim, *Cre, *Cim, *logdtB, *DskB;
    float* out; unsigned char* ws;
};

__device__ __forceinline__ float bf2f(u16 v) { return __uint_as_float(((u32)v) << 16); }
__device__ __forceinline__ u32 cvtpk(float lo, float hi) { f2 v = {lo, hi}; return __builtin_bit_cast(u32, __builtin_convertvector(v, b2v)); }
__device__ __forceinline__ float rbf(float f) { return __uint_as_float(cvtpk(f, f) << 16); }
__device__ __forceinline__ void bar() { asm volatile("s_barrier" ::: "memory"); }
template <int CTRL> __device__ __forceinline__ float dpp(float v) {
    return __builtin_bit_cast(float, __builtin_amdgcn_update_dpp(0, __builtin_bit_cast(int, v), CTRL, 0xf, 0xf, true));
}
template <int CTRL> __device__ __forceinline__ f2 dpp2(f2 v) { return (f2){dpp<CTRL>(v.x), dpp<CTRL>(v.y)}; }
__device__ __forceinline__ f2 allred16(f2 v) {
    v += dpp2<0x128>(v); v += dpp2<0x124>(v); v += dpp2<0x122>(v); v += dpp2<0x121>(v); return v;
}
#define FMA2(a, b, c) __builtin_elementwise_fma((a), (b), (c))
struct c2 { f2 r, i; };
__device__ __forceinline__ c2 cmul(c2 a, c2 b) { c2 o; o.r = FMA2(a.r, b.r, -(a.i * b.i)); o.i = FMA2(a.r, b.i, a.i * b.r); return o; }
__device__ __forceinline__ c2 cfma(c2 a, c2 b, c2 c) { c2 o; o.r = FMA2(a.r, b.r, FMA2(-a.i, b.i, c.r)); o.i = FMA2(a.r, b.i, FMA2(a.i, b.r, c.i)); return o; }
__device__ __forceinline__ void cpowf(float k, float ar2, float ai2, float& re, float& im) {
    float e = __builtin_amdgcn_exp2f(k * ar2);
    float t = __builtin_amdgcn_fractf(k * ai2);
    re = e * __builtin_amdgcn_cosf(t); im = e * __builtin_amdgcn_sinf(t);
}
__device__ __forceinline__ c2 cpow2(float k, f2 ar2, f2 ai2) {
    float r0, i0, r1, i1; cpowf(k, ar2.x, ai2.x, r0, i0); cpowf(k, ar2.y, ai2.y, r1, i1);
    c2 o; o.r = (f2){r0, r1}; o.i = (f2){i0, i1}; return o;
}

constexpr size_t WS_E    = 0;
constexpr size_t WS_FLAG = 4096;
constexpr int C_KC = 16512, C_COMP = 8256, C_NT = 2064;
constexpr int LDS_MISC = 9 * C_KC;
#define RLX_AGENT __ATOMIC_RELAXED, __HIP_MEMORY_SCOPE_AGENT
constexpr int SPIN_CAP = 100000;

__device__ __forceinline__ void load_c(const Args& a, int j, int lw, int lane, LAS unsigned char* lds, int kc) {
#pragma unroll
    for (int i = 0; i < 4; i++) {
        const int id = lw + 4 * i, comp = id >> 3, row = 8 * (id & 7) + (lane >> 3);
        const unsigned char* base = (const unsigned char*)(comp ? a.Cim : a.Cre);
        const u32 voff = (j * 64 + row) * 1152 + (((lane & 7) ^ ((row >> 1) & 7)) << 4);
        __builtin_amdgcn_global_load_lds((const GAS u32*)(base + kc * 128 + voff), (LAS u32*)(lds + kc * C_KC + id * 1024 + (id >> 1) * 16), 16, 0, 0);
    }
}

__device__ __forceinline__ void b_loader(const Args& a, int c, int g, int lw, int lane, LAS unsigned char* lds) {
    const int t0 = c * 128, p0 = g * 64;
    GAS u64* ep = (GAS u64*)(a.ws + WS_E);
    u64 ev;
    asm volatile("global_load_dwordx2 %0, %1, off sc1" : "=v"(ev) : "v"(ep) : "memory");
    const unsigned char* base[8]; u32 voff[8];
#pragma unroll
    for (int j = 0; j < 8; j++) {
        const int id = lw + 4 * j;
        const int rho = 8 * (id & 15) + (lane >> 3);
        int grow;
        if (j < 4) { grow = p0 + 16 * (rho >> 5) + (rho & 15); base[j] = (const unsigned char*)(((id >> 1) & 1) ? a.Bim : a.Bre); }
        else       { grow = t0 + 8 * (rho & 15) + (rho >> 4);   base[j] = (const unsigned char*)a.x; }
        voff[j] = grow * 1152 + (((lane & 7) ^ ((rho >> 1) & 7)) << 4);
    }
    auto issue = [&](int s) {
#pragma unroll
        for (int j = 0; j < 8; j++)
            __builtin_amdgcn_global_load_lds((const GAS u32*)(base[j] + s * 128 + voff[j]),
                                             (LAS u32*)(lds + (s & 3) * 32768 + (lw + 4 * j) * 1024), 16, 0, 0);
    };
    issue(0); issue(1); issue(2); issue(3);
    asm volatile("s_waitcnt vmcnt(24)" : "+v"(ev) :: "memory");
    const u64 tag = ev + 1;
    if (lw == 0 && lane == 0) *(LAS u64*)(lds + LDS_MISC) = tag;
    asm volatile("s_waitcnt lgkmcnt(0)" ::: "memory");
    bar();
#pragma unroll
    for (int s = 0; s < 9; s++) {
        if (s == 0) asm volatile("s_waitcnt vmcnt(16)" ::: "memory");
        else if (s + 2 <= 8) asm volatile("s_waitcnt vmcnt(8)" ::: "memory");
        else if (s == 7) asm volatile("s_waitcnt vmcnt(8)" ::: "memory");
        bar();
        if (s >= 1 && s + 3 <= 8) issue(s + 3);
        if (s == 6) { load_c(a, g, lw, lane, lds, 8); load_c(a, g, lw, lane, lds, 2); }
        if (s == 7) { load_c(a, g, lw, lane, lds, 3); load_c(a, g, lw, lane, lds, 4); }
        if (s == 8) { load_c(a, g, lw, lane, lds, 5); load_c(a, g, lw, lane, lds, 6); load_c(a, g, lw, lane, lds, 7); }
    }
    bar();
    load_c(a, g, lw, lane, lds, 0); load_c(a, g, lw, lane, lds, 1);
    bar();
    if (lw == 0) {
        const GAS u64* fl = (const GAS u64*)(a.ws + WS_FLAG + (size_t)(c * 9 + (lane < 9 ? lane : 0)) * 128);
        for (int spin = 0; spin < SPIN_CAP; spin++) {
            const u64 v = __hip_atomic_load(fl, RLX_AGENT);
            if (__all(v == tag)) break;
            __builtin_amdgcn_s_sleep(1);
        }
        if (c == 15 && g == 0 && lane == 0) __hip_atomic_store(ep, tag, RLX_AGENT);
    }
    asm volatile("s_waitcnt vmcnt(0)" ::: "memory");
    bar();
}

constexpr int WIN = 12;
__device__ __forceinline__ void b_compute(const Args& a, int c, int g, int w, int lane, LAS unsigned char* lds) {
    PROBE_DECL
    STAMP(0);
    const int n = lane & 15, q = lane >> 4;
    const int pw0 = g * 64 + 16 * w + 4 * q;
    const int pmy = pw0 + (n & 3);
    const u16 r_lr = a.Lre[pmy], r_li = a.Lim[pmy], r_dt = a.logdtB[pmy];
    f4 acc[8][2];
#pragma unroll
    for (int t = 0; t < 8; t++) { acc[t][0] = (f4){0, 0, 0, 0}; acc[t][1] = (f4){0, 0, 0, 0}; }
    const u32 sw = (u32)(n * 128 + ((q ^ ((n >> 1) & 7)) << 4));
    const u32 aW = sw + w * 4096, aX = sw + 16384;
    bf8 fw[2][2], fx[2][8];
    auto rd = [&](int buf, u32 slotoff, int ks) {
        const u32 w_ = (aW ^ (ks << 6)) + slotoff, x_ = (aX ^ (ks << 6)) + slotoff;
        fw[buf][0] = *(const LAS bf8*)(lds + w_); fw[buf][1] = *(const LAS bf8*)(lds + w_ + 2048);
#pragma unroll
        for (int t = 0; t < 8; t++) fx[buf][t] = *(const LAS bf8*)(lds + x_ + t * 2048);
    };
    auto mm = [&](int buf) {
#pragma unroll
        for (int t = 0; t < 8; t++) {
            acc[t][0] = __builtin_amdgcn_mfma_f32_16x16x32_bf16(fw[buf][0], fx[buf][t], acc[t][0], 0, 0, 0);
            acc[t][1] = __builtin_amdgcn_mfma_f32_16x16x32_bf16(fw[buf][1], fx[buf][t], acc[t][1], 0, 0, 0);
        }
    };
    f2 ar2[2], ai2[2]; c2 Lam[2], coef[2], W1[2];
    auto params = [&]() {
        const float lr = bf2f(r_lr), li = bf2f(r_li);
        const float dt = rbf(__builtin_amdgcn_exp2f(bf2f(r_dt) * 1.4426950408889634f));
        const float a2 = (dt * lr) * 1.4426950408889634f, b2 = (dt * li) * 0.15915494309189535f;
        float Lr, Li; cpowf(1.f, a2, b2, Lr, Li);
        const float nr = Lr - 1.f, ni = Li, den = __builtin_amdgcn_rcpf(lr * lr + li * li);
        const float cr = (nr * lr + ni * li) * den, ci = (ni * lr - nr * li) * den;
#define QB(dst, v) { dst[0] = (f2){dpp<0x00>(v), dpp<0x55>(v)}; dst[1] = (f2){dpp<0xAA>(v), dpp<0xFF>(v)}; }
        f2 t_[2];
        QB(ar2, a2); QB(ai2, b2);
        QB(t_, Lr); Lam[0].r = t_[0]; Lam[1].r = t_[1]; QB(t_, Li); Lam[0].i = t_[0]; Lam[1].i = t_[1];
        QB(t_, cr); coef[0].r = t_[0]; coef[1].r = t_[1]; QB(t_, ci); coef[0].i = t_[0]; coef[1].i = t_[1];
        W1[0] = cpow2((float)(8 * (15 - n)), ar2[0], ai2[0]); W1[1] = cpow2((float)(8 * (15 - n)), ar2[1], ai2[1]);
    };
    bar();
    STAMP(1);
    const u64 tag = *(const LAS u64*)(lds + LDS_MISC);
    rd(0, 0, 0);
#define ILV() { _Pragma("unroll") for (int i_ = 0; i_ < 10; i_++) { __builtin_amdgcn_sched_group_barrier(0x8, 1, 0); __builtin_amdgcn_sched_group_barrier(0x100, 1, 0); } __builtin_amdgcn_sched_group_barrier(0x8, 6, 0); }
#pragma unroll
    for (int s = 0; s < 9; s++) {
        bar();
        __builtin_amdgcn_sched_barrier(0);
        rd(1, (s & 3) * 32768, 1);
        mm(0);
        ILV();
        __builtin_amdgcn_sched_barrier(0);
        if (s < 8) rd(0, ((s + 1) & 3) * 32768, 0);
        if (s == 2) params();
        mm(1);
        if (s < 8) ILV();
        __builtin_amdgcn_sched_barrier(0);
    }
    bar();
    STAMP(2);
    const u32 tlo = (u32)tag, thi = (u32)(tag >> 32) & 0xFFu;
    c2 z[8][2];
#pragma unroll
    for (int t = 0; t < 8; t++) {
        z[t][0].r = acc[t][0].xy; z[t][1].r = acc[t][0].zw; z[t][0].i = acc[t][1].xy; z[t][1].i = acc[t][1].zw;
    }
    c2 agg[2];
#pragma unroll
    for (int h = 0; h < 2; h++) agg[h] = z[0][h];
#pragma unroll
    for (int t = 1; t < 8; t++) { agg[0] = cfma(Lam[0], agg[0], z[t][0]); agg[1] = cfma(Lam[1], agg[1], z[t][1]); }
    c2 S[2];
#pragma unroll
    for (int h = 0; h < 2; h++) { S[h] = cmul(W1[h], agg[h]); S[h].r = allred16(S[h].r); S[h].i = allred16(S[h].i); }
    const __amdgpu_buffer_rsrc_t rs = __builtin_amdgcn_make_buffer_rsrc(a.ws, 0, 0x7fffffff, 0x00020000);
    {
        const float sr = (n & 2) ? ((n & 1) ? S[1].r.y : S[1].r.x) : ((n & 1) ? S[0].r.y : S[0].r.x);
        const float si = (n & 2) ? ((n & 1) ? S[1].i.y : S[1].i.x) : ((n & 1) ? S[0].i.y : S[0].i.x);
        u4 gr; gr.x = tlo; gr.y = ((__float_as_uint(sr) + 0x80u) & 0xFFFFFF00u) | thi;
        gr.z = tlo; gr.w = ((__float_as_uint(si) + 0x80u) & 0xFFFFFF00u) | thi;
        if (n < 4) __builtin_amdgcn_raw_buffer_store_b128(gr, rs, (u32)(WS_AGG + ((size_t)c * DM + pw0 + n) * 16), 0, 16);
    }
    STAMP(3);
    const int t0 = c * 128;
    u16 xv[2][4][4]; u16 dsk[4];
#pragma unroll
    for (int nt = 0; nt < 4; nt++) {
        dsk[nt] = a.DskB[g * 64 + nt * 16 + n];
#pragma unroll
        for (int m = 0; m < 2; m++)
#pragma unroll
            for (int r = 0; r < 4; r++) xv[m][nt][r] = a.x[(size_t)(t0 + 8 * (4 * q + r) + 2 * w + m) * DM + g * 64 + nt * 16 + n];
    }
    c2 E[2];
#pragma unroll
    for (int h = 0; h < 2; h++) {
        c2 P = agg[h];
        c2 m = cpow2(8.f, ar2[h], ai2[h]);
        { c2 s; s.r = dpp2<0x111>(P.r); s.i = dpp2<0x111>(P.i); P = cfma(m, s, P); } m = cmul(m, m);
        { c2 s; s.r = dpp2<0x112>(P.r); s.i = dpp2<0x112>(P.i); P = cfma(m, s, P); } m = cmul(m, m);
        { c2 s; s.r = dpp2<0x114>(P.r); s.i = dpp2<0x114>(P.i); P = cfma(m, s, P); } m = cmul(m, m);
        { c2 s; s.r = dpp2<0x118>(P.r); s.i = dpp2<0x118>(P.i); P = cfma(m, s, P); }
        E[h].r = dpp2<0x111>(P.r); E[h].i = dpp2<0x111>(P.i);
    }
    c2 W2[2], PW[2];
#pragma unroll
    for (int h = 0; h < 2; h++) {
        W2[h] = cpow2((float)(128 * (15 - n)), ar2[h], ai2[h]);
        PW[h] = cmul(coef[h], cpow2((float)(8 * n), ar2[h], ai2[h]));
        E[h] = cmul(coef[h], E[h]);
#pragma unroll
        for (int t = 0; t < 8; t++) z[t][h] = cmul(coef[h], z[t][h]);
    }
    u32 skp[2][4][2];
#pragma unroll
    for (int m = 0; m < 2; m++)
#pragma unroll
        for (int nt = 0; nt < 4; nt++) {
            const float d = bf2f(dsk[nt]);
            skp[m][nt][0] = cvtpk(bf2f(xv[m][nt][0]) * d, bf2f(xv[m][nt][1]) * d);
            skp[m][nt][1] = cvtpk(bf2f(xv[m][nt][2]) * d, bf2f(xv[m][nt][3]) * d);
        }
    c2 X[2];
    {
        const int cp = c - 16 + n;
        const bool valid = cp >= 0;
        const u32 off = (u32)(WS_AGG + ((size_t)(valid ? cp : 0) * DM + pw0) * 16);
        u4 g0, g1, g2, g3;
        for (int spin = 0; spin < SPIN_CAP; spin++) {
            g0 = __builtin_amdgcn_raw_buffer_load_b128(rs, off, 0, 16);
            g1 = __builtin_amdgcn_raw_buffer_load_b128(rs, off + 16, 0, 16);
            g2 = __builtin_amdgcn_raw_buffer_load_b128(rs, off + 32, 0, 16);
            g3 = __builtin_amdgcn_raw_buffer_load_b128(rs, off + 48, 0, 16);
            const u32 bad = (g0.x ^ tlo) | (g0.z ^ tlo) | (g1.x ^ tlo) | (g1.z ^ tlo) | (g2.x ^ tlo) | (g2.z ^ tlo) | (g3.x ^ tlo) | (g3.z ^ tlo)
                | (((g0.y ^ thi) | (g0.w ^ thi) | (g1.y ^ thi) | (g1.w ^ thi) | (g2.y ^ thi) | (g2.w ^ thi) | (g3.y ^ thi) | (g3.w ^ thi)) & 0xFFu);
            if (__all(!valid || bad == 0)) break;
            __builtin_amdgcn_s_sleep(1);
        }
        const u32 msk = valid ? 0xFFFFFF00u : 0u;
        c2 Sp[2];
        Sp[0].r = (f2){__uint_as_float(g0.y & msk), __uint_as_float(g1.y & msk)}; Sp[0].i = (f2){__uint_as_float(g0.w & msk), __uint_as_float(g1.w & msk)};
        Sp[1].r = (f2){__uint_as_float(g2.y & msk), __uint_as_float(g3.y & msk)}; Sp[1].i = (f2){__uint_as_float(g2.w & msk), __uint_as_float(g3.w & msk)};
#pragma unroll
        for (int h = 0; h < 2; h++) { X[h] = cmul(W2[h], Sp[h]); X[h].r = allred16(X[h].r); X[h].i = allred16(X[h].i); }
    }
    STAMP(4);
    c2 hst[2];
#pragma unroll
    for (int h = 0; h < 2; h++) hst[h] = cfma(PW[h], X[h], E[h]);
    const u32 hoff = (u32)(WS_H + ((size_t)(c * 36 + g * 4 + w) * 8) * 1024 + lane * 16);
#pragma unroll
    for (int t = 0; t < 8; t++) {
        hst[0] = cfma(Lam[0], hst[0], z[t][0]); hst[1] = cfma(Lam[1], hst[1], z[t][1]);
        u4 o; o.x = cvtpk(hst[0].r.x, hst[0].r.y); o.y = cvtpk(hst[1].r.x, hst[1].r.y);
        o.z = cvtpk(-hst[0].i.x, -hst[0].i.y); o.w = cvtpk(-hst[1].i.x, -hst[1].i.y);
        __builtin_amdgcn_raw_buffer_store_b128(o, rs, hoff + t * 1024, 0, 16);
    }
    asm volatile("s_waitcnt vmcnt(0)" ::: "memory");
    STAMP(5);
    bar();
    if (w == 0 && lane == 0) __hip_atomic_store((GAS u64*)(a.ws + WS_FLAG + (size_t)(c * 9 + g) * 128), tag, RLX_AGENT);
    const u32 hb = (u32)(WS_H + ((size_t)c * 36 * 8 + 2 * w) * 1024 + lane * 16);
    u4 fa[WIN][2];
    auto kcof = [&](int i) { int k = g + i; return k >= 9 ? k - 9 : k; };
    auto ldh = [&](int ci) {
        const u32 so = (u32)(kcof(ci >> 2) * 4 + (ci & 3)) * 8192u;
        fa[ci % WIN][0] = __builtin_amdgcn_raw_buffer_load_b128(rs, hb, so, 16);
        fa[ci % WIN][1] = __builtin_amdgcn_raw_buffer_load_b128(rs, hb + 1024, so, 16);
    };
#pragma unroll
    for (int k = 0; k < 4; k++) ldh(k);
    STAMP(6);
    bar();
    STAMP(7);
#pragma unroll
    for (int k = 4; k < WIN; k++) ldh(k);
    f4 ac2[2][4];
#pragma unroll
    for (int m = 0; m < 2; m++)
#pragma unroll
        for (int nt = 0; nt < 4; nt++) ac2[m][nt] = (f4){0, 0, 0, 0};
    u32 ab[4];
#pragma unroll
    for (int cc = 0; cc < 4; cc++) ab[cc] = (n >> 3) * 1024 + (n & 7) * 128 + ((((2 * cc + (q >> 1)) ^ ((n >> 1) & 7))) << 4) + (q & 1) * 8;
    bf8 fb[2][4];
    auto rdc = [&](int ci) {
        const u32 ad = ab[ci & 3] + (u32)kcof(ci >> 2) * C_KC;
#pragma unroll
        for (int nt = 0; nt < 4; nt++) {
            const u2 re = *(const LAS u2*)(lds + ad + nt * C_NT), im = *(const LAS u2*)(lds + ad + nt * C_NT + C_COMP);
            fb[ci & 1][nt] = __builtin_bit_cast(bf8, (u4){re.x, re.y, im.x, im.y});
        }
    };
    rdc(0);
#pragma unroll
    for (int ci = 0; ci < 36; ci++) {
        __builtin_amdgcn_sched_barrier(0);
        if (ci + 1 < 36) rdc(ci + 1);
        const bf8 a0 = __builtin_bit_cast(bf8, fa[ci % WIN][0]), a1 = __builtin_bit_cast(bf8, fa[ci % WIN][1]);
#pragma unroll
        for (int nt = 0; nt < 4; nt++) {
            ac2[0][nt] = __builtin_amdgcn_mfma_f32_16x16x32_bf16(a0, fb[ci & 1][nt], ac2[0][nt], 0, 0, 0);
            ac2[1][nt] = __builtin_amdgcn_mfma_f32_16x16x32_bf16(a1, fb[ci & 1][nt], ac2[1][nt], 0, 0, 0);
        }
        if (ci + 1 < 36) { _Pragma("unroll") for (int i_ = 0; i_ < 8; i_++) { __builtin_amdgcn_sched_group_barrier(0x8, 1, 0); __builtin_amdgcn_sched_group_barrier(0x100, 1, 0); } }
        __builtin_amdgcn_sched_barrier(0);
        if (ci + WIN < 36) ldh(ci + WIN);
    }
    STAMP(8);
    const __amdgpu_buffer_rsrc_t ro = __builtin_amdgcn_make_buffer_rsrc(a.out, 0, SEQ * 2 * DM * 4, 0x00020000);
    const u32 ob = (u32)(((t0 + 32 * q + 2 * w) * (2 * DM) + DM + g * 64 + n) * 4);
#pragma unroll
    for (int m = 0; m < 2; m++)
#pragma unroll
        for (int r = 0; r < 4; r++)
#pragma unroll
            for (int nt = 0; nt < 4; nt++) {
                const u32 sk = skp[m][nt][r >> 1];
                const float v = 2.f * ac2[m][nt][r] + __uint_as_float((r & 1) ? (sk & 0xFFFF0000u) : (sk << 16));
                __builtin_amdgcn_raw_buffer_store_b32(__float_as_uint(v), ro, ob + nt * 64, (8 * r + m) * (2 * DM * 4), OUT_AUX);
            }
    STAMP(9);
    PROBE_END;
}

constexpr int UT_STRIDE = 4640;
constexpr int A_KR = 8 * UT_STRIDE;
constexpr int KR_BYTES = 8704, KV_BYTES = 1344, KR_B = 4160, TROW = 544;
constexpr int A_WAVE = KR_BYTES + KV_BYTES;

__device__ __forceinline__ u32 pk16(u32 lo, u32 hi) { return __builtin_amdgcn_perm(hi, lo, 0x05040100u); }

__device__ __forceinline__ void a_block(const Args& a, int ga, int w, int lane, LAS unsigned char* lds) {
    PROBE_DECL
    STAMP(0);
    const int d0 = ga * 8, d = d0 + w, tid = w * 64 + lane;
    u4 ur[4];
#pragma unroll
    for (int i = 0; i < 4; i++) ur[i] = *(const u4*)(a.u + (size_t)(4 * tid + i) * DM + d0);
    const u32 pA = *(const u32*)(a.logA + d * NS + 2 * lane), pI = *(const u32*)(a.Aim + d * NS + 2 * lane);
    const u32 pR = *(const u32*)(a.CreA + d * NS + 2 * lane), pC = *(const u32*)(a.CimA + d * NS + 2 * lane);
    const float dt = rbf(expf(bf2f(a.logdtA[d]))), dsk = bf2f(a.DskA[d]);
    LAS unsigned char* kr = lds + A_KR + w * A_WAVE;
    LAS unsigned char* kv = kr + KR_BYTES;
    const f2 lA = {__uint_as_float(pA << 16), __uint_as_float(pA & 0xFFFF0000u)};
    const f2 are = {-rbf(expf(lA.x)) * dt, -rbf(expf(lA.y)) * dt};
    const f2 aim = (f2){__uint_as_float(pI << 16), __uint_as_float(pI & 0xFFFF0000u)} * dt;
    const f2 a2 = are * 1.4426950408889634f, b2 = aim * 0.15915494309189535f;
    c2 Cc; Cc.r = (f2){__uint_as_float(pR << 16), __uint_as_float(pR & 0xFFFF0000u)} * dt * 2.f;
    Cc.i = (f2){__uint_as_float(pC << 16), __uint_as_float(pC & 0xFFFF0000u)} * dt * 2.f;
    const c2 w1 = cpow2(1.f, a2, b2), w8 = cpow2(8.f, a2, b2), w128 = cpow2(128.f, a2, b2);
    const c2 w2 = cmul(w1, w1), w4 = cmul(w2, w2);
    auto put = [&](int row, c2 v) { *(LAS u2*)(kr + row * TROW + lane * 8) = (u2){cvtpk(v.r.x, v.i.x), cvtpk(v.r.y, v.i.y)}; };
    const int jx = lane & 15, g4 = lane >> 4;
    const u32 fra = jx * TROW + g4 * 16;
    bf8 fr[8];
    {
        c2 v; v.r = (f2){1.f, 1.f}; v.i = (f2){0.f, 0.f};
#pragma unroll
        for (int j = 0; j < 16; j++) { c2 t; t.r = v.r; t.i = -v.i; put(j, t); if (j < 15) v = cmul(v, w8); }
#pragma unroll
        for (int s = 0; s < 8; s++) fr[s] = *(const LAS bf8*)(kr + fra + s * 64);
    }
    auto tile = [&]() {
        f4 acc = {0, 0, 0, 0};
#pragma unroll
        for (int s = 0; s < 8; s++) acc = __builtin_amdgcn_mfma_f32_16x16x32_bf16(*(const LAS bf8*)(kr + fra + s * 64), fr[s], acc, 0, 0, 0);
        return acc;
    };
    c2 G = Cc;
    {
        asm volatile("s_waitcnt lgkmcnt(0)" ::: "memory");
#pragma unroll
        for (int i = 0; i < 2; i++) {
            c2 v = G;
#pragma unroll
            for (int e = 0; e < 8; e++) { put(8 * i + e, v); if (e < 7) v = cmul(v, w1); }
            G = cmul(G, w128);
        }
        const f4 k = tile();
        *(LAS u2*)(kv + 2 * (128 * (g4 >> 1) + 8 * jx + 4 * (g4 & 1))) = (u2){cvtpk(k[0], k[1]), cvtpk(k[2], k[3])};
    }
    {
        asm volatile("s_waitcnt lgkmcnt(0)" ::: "memory");
#pragma unroll
        for (int i = 0; i < 2; i++) {
            c2 v = G;
#pragma unroll
            for (int e = 0; e < 4; e++) { put(4 * i + e, v); if (e < 3) v = cmul(v, w2); }
            G = cmul(G, w128);
        }
#pragma unroll
        for (int i = 0; i < 4; i++) { put(8 + 2 * i, G); put(9 + 2 * i, cmul(G, w4)); G = cmul(G, w128); }
        const f4 k = tile();
        const int i0 = g4 < 2 ? 256 + 64 * g4 + 4 * jx : 384 + 64 * (g4 - 2) + 2 * jx, i1 = i0 + (g4 < 2 ? 2 : 32);
        *(LAS u32*)(kv + 2 * i0) = cvtpk(k[0], k[1]); *(LAS u32*)(kv + 2 * i1) = cvtpk(k[2], k[3]);
    }
    {
        asm volatile("s_waitcnt lgkmcnt(0)" ::: "memory");
#pragma unroll
        for (int i = 0; i < 9; i++) { put(i, G); if (i < 8) G = cmul(G, w128); }
        const f4 k = tile();
        const u32 k01 = cvtpk(k[0], k[1]), k23 = cvtpk(k[2], k[3]);
        if (g4 < 2) {
            LAS u16* p = (LAS u16*)(kv + 2 * (512 + 64 * g4 + jx));
            p[0] = (u16)k01; p[16] = (u16)(k01 >> 16); p[32] = (u16)k23; p[48] = (u16)(k23 >> 16);
        } else if (lane == 32) *(LAS u16*)(kv + 2 * 640) = (u16)k01;
    }
#pragma unroll
    for (int j = 0; j < 4; j++) {
        const u32 r0 = ur[0][j], r1 = ur[1][j], r2 = ur[2][j], r3 = ur[3][j];
        *(LAS u2*)(lds + (2 * j) * UT_STRIDE + 512 + 8 * tid) = (u2){__builtin_amdgcn_perm(r1, r0, 0x05040100u), __builtin_amdgcn_perm(r3, r2, 0x05040100u)};
        *(LAS u2*)(lds + (2 * j + 1) * UT_STRIDE + 512 + 8 * tid) = (u2){__builtin_amdgcn_perm(r1, r0, 0x07060302u), __builtin_amdgcn_perm(r3, r2, 0x07060302u)};
    }
    if (lane < 32) *(LAS u4*)(lds + w * UT_STRIDE + lane * 16) = (u4){0, 0, 0, 0};
    asm volatile("s_waitcnt lgkmcnt(0)" ::: "memory");
    {
        const LAS u16* kvp = (const LAS u16*)kv;
        u32 prev7 = 0;
#pragma unroll
        for (int qq = 0; qq < 4; qq++) {
            const int c = 64 * qq + lane;
            u32 k[8];
            if (qq == 0) {
                const bool s1 = lane >= 32;
                const int base = s1 ? 256 + 4 * (lane - 32) : 8 * lane;
                const int o1[8] = {0, 0, 1, 2, 2, 2, 3, 4};
#pragma unroll
                for (int i = 0; i < 8; i++) k[i] = kvp[base + (s1 ? o1[i] : i)];
            } else if (qq == 1) {
                const int base = 384 + 2 * lane;
                const u32 v0 = kvp[base], v1 = kvp[base + 1], v2 = kvp[base + 2];
                k[0] = k[1] = k[2] = v0; k[3] = k[4] = k[5] = v1; k[6] = k[7] = v2;
            } else {
                const int base = 512 + (c - 128);
                const u32 v0 = kvp[base], v1 = kvp[base + 1];
                k[0] = k[1] = k[2] = k[3] = v0; k[4] = (lane & 1) ? v1 : v0; k[5] = k[6] = k[7] = v1;
            }
            const u32 km1 = (u32)__builtin_amdgcn_update_dpp((int)prev7, (int)k[7], 0x138, 0xf, 0xf, false);
            prev7 = (u32)__builtin_amdgcn_readlane((int)k[7], 63);
            *(LAS u4*)(kr + 16 * (255 - c)) = (u4){pk16(k[7], k[6]), pk16(k[5], k[4]), pk16(k[3], k[2]), pk16(k[1], k[0])};
            *(LAS u4*)(kr + KR_B + 16 * (255 - c)) = (u4){pk16(k[6], k[5]), pk16(k[4], k[3]), pk16(k[2], k[1]), pk16(k[0], km1)};
        }
        if (lane < 2) { *(LAS u4*)(kr + 4096 + lane * 16) = (u4){0, 0, 0, 0}; *(LAS u4*)(kr + KR_B + 4096 + lane * 16) = (u4){0, 0, 0, 0}; }
    }
    STAMP(1);
    asm volatile("s_waitcnt lgkmcnt(0)" ::: "memory");
    bar();
    STAMP(2);
    f4 acc[8];
#pragma unroll
    for (int t = 0; t < 8; t++) acc[t] = (f4){0, 0, 0, 0};
    {
        const int b = jx, kk = g4 >> 1, eh = g4 & 1;
        u32 ka = (u32)(A_KR + w * A_WAVE) + ((b & 1) ? 2 * (2047 - 16 * kk + 8 * eh - b) : KR_B + 2 * (2046 - 16 * kk + 8 * eh - b)) - 3584;
        u32 ua = (u32)(w * UT_STRIDE) + 512 + 32 * jx - 32 * kk + 16 * eh;
        for (int r = 0; r < 8; r++) {
            bf8 fu[8];
#pragma unroll
            for (int t = 0; t < 8; t++) fu[t] = *(const LAS bf8*)(lds + ua + 512 * t);
#pragma unroll
            for (int i = 0; i < 8; i++) {
                const LAS u32* p = (const LAS u32*)(lds + ka + 512 * (7 - i));
                const bf8 fk = __builtin_bit_cast(bf8, (u4){p[0], p[1], p[2], p[3]});
#pragma unroll
                for (int t = i; t < 8; t++) acc[t] = __builtin_amdgcn_mfma_f32_16x16x32_bf16(fk, fu[t - i], acc[t], 0, 0, 0);
            }
            ka -= 64; ua -= 64;
        }
    }
    STAMP(3);
#pragma unroll
    for (int t = 0; t < 8; t++) {
        LAS u2* p = (LAS u2*)(lds + w * UT_STRIDE + 512 + 2 * (256 * t + 16 * jx + 4 * g4));
        const u2 uu = *p;
        const u32 y01 = cvtpk(acc[t][0], acc[t][1]), y23 = cvtpk(acc[t][2], acc[t][3]);
        const u32 s01 = cvtpk(__uint_as_float(uu.x << 16) * dsk, __uint_as_float(uu.x & 0xFFFF0000u) * dsk);
        const u32 s23 = cvtpk(__uint_as_float(uu.y << 16) * dsk, __uint_as_float(uu.y & 0xFFFF0000u) * dsk);
        *p = (u2){cvtpk(__uint_as_float(y01 << 16) + __uint_as_float(s01 << 16), __uint_as_float(y01 & 0xFFFF0000u) + __uint_as_float(s01 & 0xFFFF0000u)),
                  cvtpk(__uint_as_float(y23 << 16) + __uint_as_float(s23 << 16), __uint_as_float(y23 & 0xFFFF0000u) + __uint_as_float(s23 & 0xFFFF0000u))};
    }
    asm volatile("s_waitcnt lgkmcnt(0)" ::: "memory");
    bar();
    STAMP(4);
    const int hf = tid & 1;
    const __amdgpu_buffer_rsrc_t ro = __builtin_amdgcn_make_buffer_rsrc(a.out, 0, SEQ * 2 * DM * 4, 0x00020000);
#pragma unroll
    for (int ps = 0; ps < 2; ps++) {
        const int rq = (tid >> 1) + 256 * ps;
        u2 v[4];
#pragma unroll
        for (int j = 0; j < 4; j++) v[j] = *(const LAS u2*)(lds + (4 * hf + j) * UT_STRIDE + 512 + 8 * rq);
        const u32 o = (u32)(((4 * rq) * (2 * DM) + d0 + 4 * hf) * 4);
        __builtin_amdgcn_raw_buffer_store_b128((u4){v[0].x << 16, v[1].x << 16, v[2].x << 16, v[3].x << 16}, ro, o, 0, OUT_AUX);
        __builtin_amdgcn_raw_buffer_store_b128((u4){v[0].x & 0xFFFF0000u, v[1].x & 0xFFFF0000u, v[2].x & 0xFFFF0000u, v[3].x & 0xFFFF0000u}, ro, o + 2 * DM * 4, 0, OUT_AUX);
        __builtin_amdgcn_raw_buffer_store_b128((u4){v[0].y << 16, v[1].y << 16, v[2].y << 16, v[3].y << 16}, ro, o + 4 * DM * 4, 0, OUT_AUX);
        __builtin_amdgcn_raw_buffer_store_b128((u4){v[0].y & 0xFFFF0000u, v[1].y & 0xFFFF0000u, v[2].y & 0xFFFF0000u, v[3].y & 0xFFFF0000u}, ro, o + 6 * DM * 4, 0, OUT_AUX);
    }
    STAMP(5);
    PROBE_END;
}

__global__ __launch_bounds__(512, 2) void kAll(Args a) {
    extern __shared__ __attribute__((aligned(16))) unsigned char smem[];
    LAS unsigned char* lds = (LAS unsigned char*)smem;
    const int b = blockIdx.x, xcd = b & 7, i = b >> 3;
    const int lane = threadIdx.x & 63, w = __builtin_amdgcn_readfirstlane(threadIdx.x >> 6);
    if (i < 18) {
        const int c = 2 * xcd + (i >= 9), g = i % 9;
        if (w < 4) b_compute(a, c, g, w, lane, lds);
        else b_loader(a, c, g, w - 4, lane, lds);
    } else a_block(a, 9 * xcd + (i - 18), w, lane, lds);
}

__global__ void k1(unsigned char* ws) { if (threadIdx.x == 0) *(u64*)(ws + (66u << 20)) = __builtin_amdgcn_s_memrealtime(); }
__global__ void k3(unsigned char* ws) {
    const u64 t3 = __builtin_amdgcn_s_memrealtime();
    const int lane = threadIdx.x;
    const u64 t1 = *(const u64*)(ws + (66u << 20));
    long long mn = 1ll << 40, mx = -(1ll << 40);
    for (int b = lane; b < 216; b += 64) { long long s = (long long)(*(const u64*)(ws + (64u << 20) + b * 16) - t1); if (s > 0 && s < 10000 && s < mn) mn = s; }
    for (int b = lane; b < 216 * 8; b += 64) { long long e = (long long)(*(const u64*)(ws + (65u << 20) + b * 8) - t1); if (e > 0 && e < 10000 && e > mx) mx = e; }
    for (int o = 32; o >= 1; o >>= 1) { long long m1 = __shfl_xor(mn, o), m2 = __shfl_xor(mx, o); mn = m1 < mn ? m1 : mn; mx = m2 > mx ? m2 : mx; }
    const long long q = PROBE3_Q == 0 ? 0 : PROBE3_Q == 1 ? mn : PROBE3_Q == 2 ? (long long)(t3 - t1) - mx : mx - mn;
    const u64 t = __builtin_amdgcn_s_memrealtime(), d = (u64)(q * PROBE_AMP);
    for (int k = 0; k < 2000000 && __builtin_amdgcn_s_memrealtime() - t < d; k++) __builtin_amdgcn_s_sleep(2);
}
extern "C" void kernel_launch(void* const* d_in, const int* in_sizes, int n_in, void* d_out, int out_size, void* d_ws, size_t ws_size, hipStream_t stream) {
    Args a;
    a.u = (const u16*)d_in[0]; a.logA = (const u16*)d_in[1]; a.Aim = (const u16*)d_in[2]; a.CreA = (const u16*)d_in[3]; a.CimA = (const u16*)d_in[4];
    a.logdtA = (const u16*)d_in[5]; a.DskA = (const u16*)d_in[6];
    a.x = (const u16*)d_in[7]; a.Lre = (const u16*)d_in[8]; a.Lim = (const u16*)d_in[9]; a.Bre = (const u16*)d_in[10]; a.Bim = (const u16*)d_in[11];
    a.Cre = (const u16*)d_in[12]; a.Cim = (const u16*)d_in[13]; a.logdtB = (const u16*)d_in[14]; a.DskB = (const u16*)d_in[15];
    a.out = (float*)d_out; a.ws = (unsigned char*)d_ws;
    (void)hipFuncSetAttribute((const void*)kAll, hipFuncAttributeMaxDynamicSharedMemorySize, LDS_BYTES);
    k1<<<1, 64, 0, stream>>>(a.ws);
    kAll<<<216, 512, LDS_BYTES, stream>>>(a);
    k3<<<1, 64, 0, stream>>>(a.ws);
}
```

```cpp
#define XX 1
#include <hip/hip_runtime.h>
#include <stdint.h>
#define GAS __attribute__((address_space(1)))
#define LAS __attribute__((address_space(3)))
typedef unsigned short u16; typedef unsigned u32; typedef unsigned long long u64;
typedef float f2 __attribute__((ext_vector_type(2)));
typedef float f4 __attribute__((ext_vector_type(4)));
typedef short bf8 __attribute__((ext_vector_type(8)));
typedef u32 u4 __attribute__((ext_vector_type(4)));
typedef u32 u2 __attribute__((ext_vector_type(2)));
typedef __bf16 b2v __attribute__((ext_vector_type(2)));

#define OUT_AUX 0
constexpr int SEQ = 2048, DM = 576, NS = 128;
constexpr size_t WS_AGG = 65536;
constexpr size_t WS_H   = 1u << 20;
constexpr int LDS_BYTES = 160 * 1024;

#define STAMP(i)
#define PROBE_DECL
#define PROBE_END

struct Args {
    const u16 *u, *logA, *Aim, *CreA, *CimA, *logdtA, *DskA;
    const u16 *x, *Lre, *Lim, *Bre, *Bim, *Cre, *Cim, *logdtB, *DskB;
    float* out; unsigned char* ws;
};

__device__ __forceinline__ float bf2f(u16 v) { return __uint_as_float(((u32)v) << 16); }
__device__ __forceinline__ u32 cvtpk(float lo, float hi) { f2 v = {lo, hi}; return __builtin_bit_cast(u32, __builtin_convertvector(v, b2v)); }
__device__ __forceinline__ float rbf(float f) { return __uint_as_float(cvtpk(f, f) << 16); }
__device__ __forceinline__ void bar() { asm volatile("s_barrier" ::: "memory"); }
template <int CTRL> __device__ __forceinline__ float dpp(float v) {
    return __builtin_bit_cast(float, __builtin_amdgcn_update_dpp(0, __builtin_bit_cast(int, v), CTRL, 0xf, 0xf, true));
}
template <int CTRL> __device__ __forceinline__ f2 dpp2(f2 v) { return (f2){dpp<CTRL>(v.x), dpp<CTRL>(v.y)}; }
__device__ __forceinline__ f2 allred16(f2 v) {
    v += dpp2<0x128>(v); v += dpp2<0x124>(v); v += dpp2<0x122>(v); v += dpp2<0x121>(v); return v;
}
#define FMA2(a, b, c) __builtin_elementwise_fma((a), (b), (c))
struct c2 { f2 r, i; };
__device__ __forceinline__ c2 cmul(c2 a, c2 b) { c2 o; o.r = FMA2(a.r, b.r, -(a.i * b.i)); o.i = FMA2(a.r, b.i, a.i * b.r); return o; }
__device__ __forceinline__ c2 cfma(c2 a, c2 b, c2 c) { c2 o; o.r = FMA2(a.r, b.r, FMA2(-a.i, b.i, c.r)); o.i = FMA2(a.r, b.i, FMA2(a.i, b.r, c.i)); return o; }
__device__ __forceinline__ void cpowf(float k, float ar2, float ai2, float& re, float& im) {
    float e = __builtin_amdgcn_exp2f(k * ar2);
    float t = __builtin_amdgcn_fractf(k * ai2);
    re = e * __builtin_amdgcn_cosf(t); im = e * __builtin_amdgcn_sinf(t);
}
__device__ __forceinline__ c2 cpow2(float k, f2 ar2, f2 ai2) {
    float r0, i0, r1, i1; cpowf(k, ar2.x, ai2.x, r0, i0); cpowf(k, ar2.y, ai2.y, r1, i1);
    c2 o; o.r = (f2){r0, r1}; o.i = (f2){i0, i1}; return o;
}

constexpr size_t WS_E    = 0;
constexpr size_t WS_FLAG = 4096;
constexpr int C_KC = 16512, C_COMP = 8256, C_NT = 2064;
constexpr int LDS_MISC = 9 * C_KC;
#define RLX_AGENT __ATOMIC_RELAXED, __HIP_MEMORY_SCOPE_AGENT
constexpr int SPIN_CAP = 100000;

__device__ __forceinline__ void load_c(const Args& a, int j, int lw, int lane, LAS unsigned char* lds, int kc) {
#pragma unroll
    for (int i = 0; i < 4; i++) {
        const int id = lw + 4 * i, comp = id >> 3, row = 8 * (id & 7) + (lane >> 3);
        const unsigned char* base = (const unsigned char*)(comp ? a.Cim : a.Cre);
        const u32 voff = (j * 64 + row) * 1152 + (((lane & 7) ^ ((row >> 1) & 7)) << 4);
        __builtin_amdgcn_global_load_lds((const GAS u32*)(base + kc * 128 + voff), (LAS u32*)(lds + kc * C_KC + id * 1024 + (id >> 1) * 16), 16, 0, 0);
    }
}

__device__ __forceinline__ void b_loader(const Args& a, int c, int g, int lw, int lane, LAS unsigned char* lds) {
    const int t0 = c * 128, p0 = g * 64;
    GAS u64* ep = (GAS u64*)(a.ws + WS_E);
    u64 ev;
    asm volatile("global_load_dwordx2 %0, %1, off sc1" : "=v"(ev) : "v"(ep) : "memory");
    const unsigned char* base[8]; u32 voff[8];
#pragma unroll
    for (int j = 0; j < 8; j++) {
        const int id = lw + 4 * j;
        const int rho = 8 * (id & 15) + (lane >> 3);
        int grow;
        if (j < 4) { grow = p0 + 16 * (rho >> 5) + (rho & 15); base[j] = (const unsigned char*)(((id >> 1) & 1) ? a.Bim : a.Bre); }
        else       { grow = t0 + 8 * (rho & 15) + (rho >> 4);   base[j] = (const unsigned char*)a.x; }
        voff[j] = grow * 1152 + (((lane & 7) ^ ((rho >> 1) & 7)) << 4);
    }
    auto issue = [&](int s) {
#pragma unroll
        for (int j = 0; j < 8; j++)
            __builtin_amdgcn_global_load_lds((const GAS u32*)(base[j] + s * 128 + voff[j]),
                                             (LAS u32*)(lds + (s & 3) * 32768 + (lw + 4 * j) * 1024), 16, 0, 0);
    };
    issue(0); issue(1); issue(2); issue(3);
    asm volatile("s_waitcnt vmcnt(24)" : "+v"(ev) :: "memory");
    const u64 tag = ev + 1;
    if (lw == 0 && lane == 0) *(LAS u64*)(lds + LDS_MISC) = tag;
    asm volatile("s_waitcnt lgkmcnt(0)" ::: "memory");
    bar();
#pragma unroll
    for (int s = 0; s < 9; s++) {
        if (s == 0) asm volatile("s_waitcnt vmcnt(16)" ::: "memory");
        else if (s + 2 <= 8) asm volatile("s_waitcnt vmcnt(8)" ::: "memory");
        else if (s == 7) asm volatile("s_waitcnt vmcnt(8)" ::: "memory");
        bar();
        if (s >= 1 && s + 3 <= 8) issue(s + 3);
        if (s == 6) { load_c(a, g, lw, lane, lds, 8); load_c(a, g, lw, lane, lds, 2); }
        if (s == 7) { load_c(a, g, lw, lane, lds, 3); load_c(a, g, lw, lane, lds, 4); }
        if (s == 8) { load_c(a, g, lw, lane, lds, 5); load_c(a, g, lw, lane, lds, 6); load_c(a, g, lw, lane, lds, 7); }
    }
    bar();
    load_c(a, g, lw, lane, lds, 0); load_c(a, g, lw, lane, lds, 1);
    bar();
    if (lw == 0) {
        const GAS u64* fl = (const GAS u64*)(a.ws + WS_FLAG + (size_t)(c * 9 + (lane < 9 ? lane : 0)) * 128);
        for (int spin = 0; spin < SPIN_CAP; spin++) {
            const u64 v = __hip_atomic_load(fl, RLX_AGENT);
            if (__all(v == tag)) break;
            __builtin_amdgcn_s_sleep(1);
        }
        if (c == 15 && g == 0 && lane == 0) __hip_atomic_store(ep, tag, RLX_AGENT);
    }
    asm volatile("s_waitcnt vmcnt(0)" ::: "memory");
    bar();
}

constexpr int WIN = 12;
__device__ __forceinline__ void b_compute(const Args& a, int c, int g, int w, int lane, LAS unsigned char* lds) {
    PROBE_DECL
    STAMP(0);
    const int n = lane & 15, q = lane >> 4;
    const int pw0 = g * 64 + 16 * w + 4 * q;
    const int pmy = pw0 + (n & 3);
    const u16 r_lr = a.Lre[pmy], r_li = a.Lim[pmy], r_dt = a.logdtB[pmy];
    f4 acc[8][2];
#pragma unroll
    for (int t = 0; t < 8; t++) { acc[t][0] = (f4){0, 0, 0, 0}; acc[t][1] = (f4){0, 0, 0, 0}; }
    const u32 sw = (u32)(n * 128 + ((q ^ ((n >> 1) & 7)) << 4));
    const u32 aW = sw + w * 4096, aX = sw + 16384;
    bf8 fw[2][2], fx[2][8];
    auto rd = [&](int buf, u32 slotoff, int ks) {
        const u32 w_ = (aW ^ (ks << 6)) + slotoff, x_ = (aX ^ (ks << 6)) + slotoff;
        fw[buf][0] = *(const LAS bf8*)(lds + w_); fw[buf][1] = *(const LAS bf8*)(lds + w_ + 2048);
#pragma unroll
        for (int t = 0; t < 8; t++) fx[buf][t] = *(const LAS bf8*)(lds + x_ + t * 2048);
    };
    auto mm = [&](int buf) {
#pragma unroll
        for (int t = 0; t < 8; t++) {
            acc[t][0] = __builtin_amdgcn_mfma_f32_16x16x32_bf16(fw[buf][0], fx[buf][t], acc[t][0], 0, 0, 0);
            acc[t][1] = __builtin_amdgcn_mfma_f32_16x16x32_bf16(fw[buf][1], fx[buf][t], acc[t][1], 0, 0, 0);
        }
    };
    f2 ar2[2], ai2[2]; c2 Lam[2], coef[2], W1[2];
    auto params = [&]() {
        const float lr = bf2f(r_lr), li = bf2f(r_li);
        const float dt = rbf(__builtin_amdgcn_exp2f(bf2f(r_dt) * 1.4426950408889634f));
        const float a2 = (dt * lr) * 1.4426950408889634f, b2 = (dt * li) * 0.15915494309189535f;
        float Lr, Li; cpowf(1.f, a2, b2, Lr, Li);
        const float nr = Lr - 1.f, ni = Li, den = __builtin_amdgcn_rcpf(lr * lr + li * li);
        const float cr = (nr * lr + ni * li) * den, ci = (ni * lr - nr * li) * den;
#define QB(dst, v) { dst[0] = (f2){dpp<0x00>(v), dpp<0x55>(v)}; dst[1] = (f2){dpp<0xAA>(v), dpp<0xFF>(v)}; }
        f2 t_[2];
        QB(ar2, a2); QB(ai2, b2);
        QB(t_, Lr); Lam[0].r = t_[0]; Lam[1].r = t_[1]; QB(t_, Li); Lam[0].i = t_[0]; Lam[1].i = t_[1];
        QB(t_, cr); coef[0].r = t_[0]; coef[1].r = t_[1]; QB(t_, ci); coef[0].i = t_[0]; coef[1].i = t_[1];
        W1[0] = cpow2((float)(8 * (15 - n)), ar2[0], ai2[0]); W1[1] = cpow2((float)(8 * (15 - n)), ar2[1], ai2[1]);
    };
    bar();
    STAMP(1);
    const u64 tag = *(const LAS u64*)(lds + LDS_MISC);
    rd(0, 0, 0);
#define ILV() { _Pragma("unroll") for (int i_ = 0; i_ < 10; i_++) { __builtin_amdgcn_sched_group_barrier(0x8, 1, 0); __builtin_amdgcn_sched_group_barrier(0x100, 1, 0); } __builtin_amdgcn_sched_group_barrier(0x8, 6, 0); }
#pragma unroll
    for (int s = 0; s < 9; s++) {
        bar();
        __builtin_amdgcn_sched_barrier(0);
        rd(1, (s & 3) * 32768, 1);
        mm(0);
        ILV();
        __builtin_amdgcn_sched_barrier(0);
        if (s < 8) rd(0, ((s + 1) & 3) * 32768, 0);
        if (s == 2) params();
        mm(1);
        if (s < 8) ILV();
        __builtin_amdgcn_sched_barrier(0);
    }
    bar();
    STAMP(2);
    const u32 tlo = (u32)tag, thi = (u32)(tag >> 32) & 0xFFu;
    c2 z[8][2];
#pragma unroll
    for (int t = 0; t < 8; t++) {
        z[t][0].r = acc[t][0].xy; z[t][1].r = acc[t][0].zw; z[t][0].i = acc[t][1].xy; z[t][1].i = acc[t][1].zw;
    }
    c2 agg[2];
#pragma unroll
    for (int h = 0; h < 2; h++) agg[h] = z[0][h];
#pragma unroll
    for (int t = 1; t < 8; t++) { agg[0] = cfma(Lam[0], agg[0], z[t][0]); agg[1] = cfma(Lam[1], agg[1], z[t][1]); }
    c2 S[2];
#pragma unroll
    for (int h = 0; h < 2; h++) { S[h] = cmul(W1[h], agg[h]); S[h].r = allred16(S[h].r); S[h].i = allred16(S[h].i); }
    const __amdgpu_buffer_rsrc_t rs = __builtin_amdgcn_make_buffer_rsrc(a.ws, 0, 0x7fffffff, 0x00020000);
    {
        const float sr = (n & 2) ? ((n & 1) ? S[1].r.y : S[1].r.x) : ((n & 1) ? S[0].r.y : S[0].r.x);
        const float si = (n & 2) ? ((n & 1) ? S[1].i.y : S[1].i.x) : ((n & 1) ? S[0].i.y : S[0].i.x);
        u4 gr; gr.x = tlo; gr.y = ((__float_as_uint(sr) + 0x80u) & 0xFFFFFF00u) | thi;
        gr.z = tlo; gr.w = ((__float_as_uint(si) + 0x80u) & 0xFFFFFF00u) | thi;
        if (n < 4) __builtin_amdgcn_raw_buffer_store_b128(gr, rs, (u32)(WS_AGG + ((size_t)c * DM + pw0 + n) * 16), 0, 16);
    }
    STAMP(3);
    const int t0 = c * 128;
    u16 xv[2][4][4]; u16 dsk[4];
#pragma unroll
    for (int nt = 0; nt < 4; nt++) {
        dsk[nt] = a.DskB[g * 64 + nt * 16 + n];
#pragma unroll
        for (int m = 0; m < 2; m++)
#pragma unroll
            for (int r = 0; r < 4; r++) xv[m][nt][r] = a.x[(size_t)(t0 + 8 * (4 * q + r) + 2 * w + m) * DM + g * 64 + nt * 16 + n];
    }
    c2 E[2];
#pragma unroll
    for (int h = 0; h < 2; h++) {
        c2 P = agg[h];
        c2 m = cpow2(8.f, ar2[h], ai2[h]);
        { c2 s; s.r = dpp2<0x111>(P.r); s.i = dpp2<0x111>(P.i); P = cfma(m, s, P); } m = cmul(m, m);
        { c2 s; s.r = dpp2<0x112>(P.r); s.i = dpp2<0x112>(P.i); P = cfma(m, s, P); } m = cmul(m, m);
        { c2 s; s.r = dpp2<0x114>(P.r); s.i = dpp2<0x114>(P.i); P = cfma(m, s, P); } m = cmul(m, m);
        { c2 s; s.r = dpp2<0x118>(P.r); s.i = dpp2<0x118>(P.i); P = cfma(m, s, P); }
        E[h].r = dpp2<0x111>(P.r); E[h].i = dpp2<0x111>(P.i);
    }
    c2 W2[2], PW[2];
#pragma unroll
    for (int h = 0; h < 2; h++) {
        W2[h] = cpow2((float)(128 * (15 - n)), ar2[h], ai2[h]);
        PW[h] = cmul(coef[h], cpow2((float)(8 * n), ar2[h], ai2[h]));
        E[h] = cmul(coef[h], E[h]);
#pragma unroll
        for (int t = 0; t < 8; t++) z[t][h] = cmul(coef[h], z[t][h]);
    }
    u32 skp[2][4][2];
#pragma unroll
    for (int m = 0; m < 2; m++)
#pragma unroll
        for (int nt = 0; nt < 4; nt++) {
            const float d = bf2f(dsk[nt]);
            skp[m][nt][0] = cvtpk(bf2f(xv[m][nt][0]) * d, bf2f(xv[m][nt][1]) * d);
            skp[m][nt][1] = cvtpk(bf2f(xv[m][nt][2]) * d, bf2f(xv[m][nt][3]) * d);
        }
    c2 X[2];
    {
        const int cp = c - 16 + n;
        const bool valid = cp >= 0;
        const u32 off = (u32)(WS_AGG + ((size_t)(valid ? cp : 0) * DM + pw0) * 16);
        u4 g0, g1, g2, g3;
        for (int spin = 0; spin < SPIN_CAP; spin++) {
            g0 = __builtin_amdgcn_raw_buffer_load_b128(rs, off, 0, 16);
            g1 = __builtin_amdgcn_raw_buffer_load_b128(rs, off + 16, 0, 16);
            g2 = __builtin_amdgcn_raw_buffer_load_b128(rs, off + 32, 0, 16);
            g3 = __builtin_amdgcn_raw_buffer_load_b128(rs, off + 48, 0, 16);
            const u32 bad = (g0.x ^ tlo) | (g0.z ^ tlo) | (g1.x ^ tlo) | (g1.z ^ tlo) | (g2.x ^ tlo) | (g2.z ^ tlo) | (g3.x ^ tlo) | (g3.z ^ tlo)
                | (((g0.y ^ thi) | (g0.w ^ thi) | (g1.y ^ thi) | (g1.w ^ thi) | (g2.y ^ thi) | (g2.w ^ thi) | (g3.y ^ thi) | (g3.w ^ thi)) & 0xFFu);
            if (__all(!valid || bad == 0)) break;
            __builtin_amdgcn_s_sleep(1);
        }
        const u32 msk = valid ? 0xFFFFFF00u : 0u;
        c2 Sp[2];
        Sp[0].r = (f2){__uint_as_float(g0.y & msk), __uint_as_float(g1.y & msk)}; Sp[0].i = (f2){__uint_as_float(g0.w & msk), __uint_as_float(g1.w & msk)};
        Sp[1].r = (f2){__uint_as_float(g2.y & msk), __uint_as_float(g3.y & msk)}; Sp[1].i = (f2){__uint_as_float(g2.w & msk), __uint_as_float(g3.w & msk)};
#pragma unroll
        for (int h = 0; h < 2; h++) { X[h] = cmul(W2[h], Sp[h]); X[h].r = allred16(X[h].r); X[h].i = allred16(X[h].i); }
    }
    STAMP(4);
    c2 hst[2];
#pragma unroll
    for (int h = 0; h < 2; h++) hst[h] = cfma(PW[h], X[h], E[h]);
    const u32 hoff = (u32)(WS_H + ((size_t)(c * 36 + g * 4 + w) * 8) * 1024 + lane * 16);
#pragma unroll
    for (int t = 0; t < 8; t++) {
        hst[0] = cfma(Lam[0], hst[0], z[t][0]); hst[1] = cfma(Lam[1], hst[1], z[t][1]);
        u4 o; o.x = cvtpk(hst[0].r.x, hst[0].r.y); o.y = cvtpk(hst[1].r.x, hst[1].r.y);
        o.z = cvtpk(-hst[0].i.x, -hst[0].i.y); o.w = cvtpk(-hst[1].i.x, -hst[1].i.y);
        __builtin_amdgcn_raw_buffer_store_b128(o, rs, hoff + t * 1024, 0, 16);
    }
    asm volatile("s_waitcnt vmcnt(0)" ::: "memory");
    STAMP(5);
    bar();
    if (w == 0 && lane == 0) __hip_atomic_store((GAS u64*)(a.ws + WS_FLAG + (size_t)(c * 9 + g) * 128), tag, RLX_AGENT);
    const u32 hb = (u32)(WS_H + ((size_t)c * 36 * 8 + 2 * w) * 1024 + lane * 16);
    u4 fa[WIN][2];
    auto kcof = [&](int i) { int k = g + i; return k >= 9 ? k - 9 : k; };
    auto ldh = [&](int ci) {
        const u32 so = (u32)(kcof(ci >> 2) * 4 + (ci & 3)) * 8192u;
        fa[ci % WIN][0] = __builtin_amdgcn_raw_buffer_load_b128(rs, hb, so, 16);
        fa[ci % WIN][1] = __builtin_amdgcn_raw_buffer_load_b128(rs, hb + 1024, so, 16);
    };
#pragma unroll
    for (int k = 0; k < 4; k++) ldh(k);
    STAMP(6);
    bar();
    STAMP(7);
#pragma unroll
    for (int k = 4; k < WIN; k++) ldh(k);
    f4 ac2[2][4];
#pragma unroll
    for (int m = 0; m < 2; m++)
#pragma unroll
        for (int nt = 0; nt < 4; nt++) ac2[m][nt] = (f4){0, 0, 0, 0};
    u32 ab[4];
#pragma unroll
    for (int cc = 0; cc < 4; cc++) ab[cc] = (n >> 3) * 1024 + (n & 7) * 128 + ((((2 * cc + (q >> 1)) ^ ((n >> 1) & 7))) << 4) + (q & 1) * 8;
    bf8 fb[2][4];
    auto rdc = [&](int ci) {
        const u32 ad = ab[ci & 3] + (u32)kcof(ci >> 2) * C_KC;
#pragma unroll
        for (int nt = 0; nt < 4; nt++) {
            const u2 re = *(const LAS u2*)(lds + ad + nt * C_NT), im = *(const LAS u2*)(lds + ad + nt * C_NT + C_COMP);
            fb[ci & 1][nt] = __builtin_bit_cast(bf8, (u4){re.x, re.y, im.x, im.y});
        }
    };
    rdc(0);
#pragma unroll
    for (int ci = 0; ci < 36; ci++) {
        __builtin_amdgcn_sched_barrier(0);
        if (ci + 1 < 36) rdc(ci + 1);
        const bf8 a0 = __builtin_bit_cast(bf8, fa[ci % WIN][0]), a1 = __builtin_bit_cast(bf8, fa[ci % WIN][1]);
#pragma unroll
        for (int nt = 0; nt < 4; nt++) {
            ac2[0][nt] = __builtin_amdgcn_mfma_f32_16x16x32_bf16(a0, fb[ci & 1][nt], ac2[0][nt], 0, 0, 0);
            ac2[1][nt] = __builtin_amdgcn_mfma_f32_16x16x32_bf16(a1, fb[ci & 1][nt], ac2[1][nt], 0, 0, 0);
        }
        if (ci + 1 < 36) { _Pragma("unroll") for (int i_ = 0; i_ < 8; i_++) { __builtin_amdgcn_sched_group_barrier(0x8, 1, 0); __builtin_amdgcn_sched_group_barrier(0x100, 1, 0); } }
        __builtin_amdgcn_sched_barrier(0);
        if (ci + WIN < 36) ldh(ci + WIN);
    }
    STAMP(8);
    const __amdgpu_buffer_rsrc_t ro = __builtin_amdgcn_make_buffer_rsrc(a.out, 0, SEQ * 2 * DM * 4, 0x00020000);
    const u32 ob = (u32)(((t0 + 32 * q + 2 * w) * (2 * DM) + DM + g * 64 + n) * 4);
#pragma unroll
    for (int m = 0; m < 2; m++)
#pragma unroll
        for (int r = 0; r < 4; r++)
#pragma unroll
            for (int nt = 0; nt < 4; nt++) {
                const u32 sk = skp[m][nt][r >> 1];
                const float v = 2.f * ac2[m][nt][r] + __uint_as_float((r & 1) ? (sk & 0xFFFF0000u) : (sk << 16));
                __builtin_amdgcn_raw_buffer_store_b32(__float_as_uint(v), ro, ob + nt * 64, (8 * r + m) * (2 * DM * 4), OUT_AUX);
            }
    STAMP(9);
    PROBE_END;
}

constexpr int UT_STRIDE = 4640;
constexpr int A_KR = 8 * UT_STRIDE;
constexpr int KR_BYTES = 8704, KV_BYTES = 1344, KR_B = 4160, TROW = 544;
constexpr int A_WAVE = KR_BYTES + KV_BYTES;

__device__ __forceinline__ u32 pk16(u32 lo, u32 hi) { return __builtin_amdgcn_perm(hi, lo, 0x05040100u); }

__device__ __forceinline__ void a_block(const Args& a, int ga, int w, int lane, LAS unsigned char* lds) {
    PROBE_DECL
    STAMP(0);
    const int d0 = ga * 8, d = d0 + w, tid = w * 64 + lane;
    u4 ur[4];
#pragma unroll
    for (int i = 0; i < 4; i++) ur[i] = *(const u4*)(a.u + (size_t)(4 * tid + i) * DM + d0);
    const u32 pA = *(const u32*)(a.logA + d * NS + 2 * lane), pI = *(const u32*)(a.Aim + d * NS + 2 * lane);
    const u32 pR = *(const u32*)(a.CreA + d * NS + 2 * lane), pC = *(const u32*)(a.CimA + d * NS + 2 * lane);
    const float dt = rbf(expf(bf2f(a.logdtA[d]))), dsk = bf2f(a.DskA[d]);
    LAS unsigned char* kr = lds + A_KR + w * A_WAVE;
    LAS unsigned char* kv = kr + KR_BYTES;
    const f2 lA = {__uint_as_float(pA << 16), __uint_as_float(pA & 0xFFFF0000u)};
    const f2 are = {-rbf(expf(lA.x)) * dt, -rbf(expf(lA.y)) * dt};
    const f2 aim = (f2){__uint_as_float(pI << 16), __uint_as_float(pI & 0xFFFF0000u)} * dt;
    const f2 a2 = are * 1.4426950408889634f, b2 = aim * 0.15915494309189535f;
    c2 Cc; Cc.r = (f2){__uint_as_float(pR << 16), __uint_as_float(pR & 0xFFFF0000u)} * dt * 2.f;
    Cc.i = (f2){__uint_as_float(pC << 16), __uint_as_float(pC & 0xFFFF0000u)} * dt * 2.f;
    const c2 w1 = cpow2(1.f, a2, b2), w8 = cpow2(8.f, a2, b2), w128 = cpow2(128.f, a2, b2);
    const c2 w2 = cmul(w1, w1), w4 = cmul(w2, w2);
    auto put = [&](int row, c2 v) { *(LAS u2*)(kr + row * TROW + lane * 8) = (u2){cvtpk(v.r.x, v.i.x), cvtpk(v.r.y, v.i.y)}; };
    const int jx = lane & 15, g4 = lane >> 4;
    const u32 fra = jx * TROW + g4 * 16;
    bf8 fr[8];
    {
        c2 v; v.r = (f2){1.f, 1.f}; v.i = (f2){0.f, 0.f};
#pragma unroll
        for (int j = 0; j < 16; j++) { c2 t; t.r = v.r; t.i = -v.i; put(j, t); if (j < 15) v = cmul(v, w8); }
#pragma unroll
        for (int s = 0; s < 8; s++) fr[s] = *(const LAS bf8*)(kr + fra + s * 64);
    }
    auto tile = [&]() {
        f4 acc = {0, 0, 0, 0};
#pragma unroll
        for (int s = 0; s < 8; s++) acc = __builtin_amdgcn_mfma_f32_16x16x32_bf16(*(const LAS bf8*)(kr + fra + s * 64), fr[s], acc, 0, 0, 0);
        return acc;
    };
    c2 G = Cc;
    {
        asm volatile("s_waitcnt lgkmcnt(0)" ::: "memory");
#pragma unroll
        for (int i = 0; i < 2; i++) {
            c2 v = G;
#pragma unroll
            for (int e = 0; e < 8; e++) { put(8 * i + e, v); if (e < 7) v = cmul(v, w1); }
            G = cmul(G, w128);
        }
        const f4 k = tile();
        *(LAS u2*)(kv + 2 * (128 * (g4 >> 1) + 8 * jx + 4 * (g4 & 1))) = (u2){cvtpk(k[0], k[1]), cvtpk(k[2], k[3])};
    }
    {
        asm volatile("s_waitcnt lgkmcnt(0)" ::: "memory");
#pragma unroll
        for (int i = 0; i < 2; i++) {
            c2 v = G;
#pragma unroll
            for (int e = 0; e < 4; e++) { put(4 * i + e, v); if (e < 3) v = cmul(v, w2); }
            G = cmul(G, w128);
        }
#pragma unroll
        for (int i = 0; i < 4; i++) { put(8 + 2 * i, G); put(9 + 2 * i, cmul(G, w4)); G = cmul(G, w128); }
        const f4 k = tile();
        const int i0 = g4 < 2 ? 256 + 64 * g4 + 4 * jx : 384 + 64 * (g4 - 2) + 2 * jx, i1 = i0 + (g4 < 2 ? 2 : 32);
        *(LAS u32*)(kv + 2 * i0) = cvtpk(k[0], k[1]); *(LAS u32*)(kv + 2 * i1) = cvtpk(k[2], k[3]);
    }
    {
        asm volatile("s_waitcnt lgkmcnt(0)" ::: "memory");
#pragma unroll
        for (int i = 0; i < 9; i++) { put(i, G); if (i < 8) G = cmul(G, w128); }
        const f4 k = tile();
        const u32 k01 = cvtpk(k[0], k[1]), k23 = cvtpk(k[2], k[3]);
        if (g4 < 2) {
            LAS u16* p = (LAS u16*)(kv + 2 * (512 + 64 * g4 + jx));
            p[0] = (u16)k01; p[16] = (u16)(k01 >> 16); p[32] = (u16)k23; p[48] = (u16)(k23 >> 16);
        } else if (lane == 32) *(LAS u16*)(kv + 2 * 640) = (u16)k01;
    }
#pragma unroll
    for (int j = 0; j < 4; j++) {
        const u32 r0 = ur[0][j], r1 = ur[1][j], r2 = ur[2][j], r3 = ur[3][j];
        *(LAS u2*)(lds + (2 * j) * UT_STRIDE + 512 + 8 * tid) = (u2){__builtin_amdgcn_perm(r1, r0, 0x05040100u), __builtin_amdgcn_perm(r3, r2, 0x05040100u)};
        *(LAS u2*)(lds + (2 * j + 1) * UT_STRIDE + 512 + 8 * tid) = (u2){__builtin_amdgcn_perm(r1, r0, 0x07060302u), __builtin_amdgcn_perm(r3, r2, 0x07060302u)};
    }
    if (lane < 32) *(LAS u4*)(lds + w * UT_STRIDE + lane * 16) = (u4){0, 0, 0, 0};
    asm volatile("s_waitcnt lgkmcnt(0)" ::: "memory");
    {
        const LAS u16* kvp = (const LAS u16*)kv;
        u32 prev7 = 0;
#pragma unroll
        for (int qq = 0; qq < 4; qq++) {
            const int c = 64 * qq + lane;
            u32 k[8];
            if (qq == 0) {
                const bool s1 = lane >= 32;
                const int base = s1 ? 256 + 4 * (lane - 32) : 8 * lane;
                const int o1[8] = {0, 0, 1, 2, 2, 2, 3, 4};
#pragma unroll
                for (int i = 0; i < 8; i++) k[i] = kvp[base + (s1 ? o1[i] : i)];
            } else if (qq == 1) {
                const int base = 384 + 2 * lane;
                const u32 v0 = kvp[base], v1 = kvp[base + 1], v2 = kvp[base + 2];
                k[0] = k[1] = k[2] = v0; k[3] = k[4] = k[5] = v1; k[6] = k[7] = v2;
            } else {
                const int base = 512 + (c - 128);
                const u32 v0 = kvp[base], v1 = kvp[base + 1];
                k[0] = k[1] = k[2] = k[3] = v0; k[4] = (lane & 1) ? v1 : v0; k[5] = k[6] = k[7] = v1;
            }
            const u32 km1 = (u32)__builtin_amdgcn_update_dpp((int)prev7, (int)k[7], 0x138, 0xf, 0xf, false);
            prev7 = (u32)__builtin_amdgcn_readlane((int)k[7], 63);
            *(LAS u4*)(kr + 16 * (255 - c)) = (u4){pk16(k[7], k[6]), pk16(k[5], k[4]), pk16(k[3], k[2]), pk16(k[1], k[0])};
            *(LAS u4*)(kr + KR_B + 16 * (255 - c)) = (u4){pk16(k[6], k[5]), pk16(k[4], k[3]), pk16(k[2], k[1]), pk16(k[0], km1)};
        }
        if (lane < 2) { *(LAS u4*)(kr + 4096 + lane * 16) = (u4){0, 0, 0, 0}; *(LAS u4*)(kr + KR_B + 4096 + lane * 16) = (u4){0, 0, 0, 0}; }
    }
    STAMP(1);
    asm volatile("s_waitcnt lgkmcnt(0)" ::: "memory");
    bar();
    STAMP(2);
    f4 acc[8];
#pragma unroll
    for (int t = 0; t < 8; t++) acc[t] = (f4){0, 0, 0, 0};
    {
        const int b = jx, kk = g4 >> 1, eh = g4 & 1;
        u32 ka = (u32)(A_KR + w * A_WAVE) + ((b & 1) ? 2 * (2047 - 16 * kk + 8 * eh - b) : KR_B + 2 * (2046 - 16 * kk + 8 * eh - b)) - 3584;
        u32 ua = (u32)(w * UT_STRIDE) + 512 + 32 * jx - 32 * kk + 16 * eh;
        for (int r = 0; r < 8; r++) {
            bf8 fu[8];
#pragma unroll
            for (int t = 0; t < 8; t++) fu[t] = *(const LAS bf8*)(lds + ua + 512 * t);
#pragma unroll
            for (int i = 0; i < 8; i++) {
                const LAS u32* p = (const LAS u32*)(lds + ka + 512 * (7 - i));
                const bf8 fk = __builtin_bit_cast(bf8, (u4){p[0], p[1], p[2], p[3]});
#pragma unroll
                for (int t = i; t < 8; t++) acc[t] = __builtin_amdgcn_mfma_f32_16x16x32_bf16(fk, fu[t - i], acc[t], 0, 0, 0);
            }
            ka -= 64; ua -= 64;
        }
    }
    STAMP(3);
#pragma unroll
    for (int t = 0; t < 8; t++) {
        LAS u2* p = (LAS u2*)(lds + w * UT_STRIDE + 512 + 2 * (256 * t + 16 * jx + 4 * g4));
        const u2 uu = *p;
        const u32 y01 = cvtpk(acc[t][0], acc[t][1]), y23 = cvtpk(acc[t][2], acc[t][3]);
        const u32 s01 = cvtpk(__uint_as_float(uu.x << 16) * dsk, __uint_as_float(uu.x & 0xFFFF0000u) * dsk);
        const u32 s23 = cvtpk(__uint_as_float(uu.y << 16) * dsk, __uint_as_float(uu.y & 0xFFFF0000u) * dsk);
        *p = (u2){cvtpk(__uint_as_float(y01 << 16) + __uint_as_float(s01 << 16), __uint_as_float(y01 & 0xFFFF0000u) + __uint_as_float(s01 & 0xFFFF0000u)),
                  cvtpk(__uint_as_float(y23 << 16) + __uint_as_float(s23 << 16), __uint_as_float(y23 & 0xFFFF0000u) + __uint_as_float(s23 & 0xFFFF0000u))};
    }
    asm volatile("s_waitcnt lgkmcnt(0)" ::: "memory");
    bar();
    STAMP(4);
    const int hf = tid & 1;
    const __amdgpu_buffer_rsrc_t ro = __builtin_amdgcn_make_buffer_rsrc(a.out, 0, SEQ * 2 * DM * 4, 0x00020000);
#pragma unroll
    for (int ps = 0; ps < 2; ps++) {
        const int rq = (tid >> 1) + 256 * ps;
        u2 v[4];
#pragma unroll
        for (int j = 0; j < 4; j++) v[j] = *(const LAS u2*)(lds + (4 * hf + j) * UT_STRIDE + 512 + 8 * rq);
        const u32 o = (u32)(((4 * rq) * (2 * DM) + d0 + 4 * hf) * 4);
        __builtin_amdgcn_raw_buffer_store_b128((u4){v[0].x << 16, v[1].x << 16, v[2].x << 16, v[3].x << 16}, ro, o, 0, OUT_AUX);
        __builtin_amdgcn_raw_buffer_store_b128((u4){v[0].x & 0xFFFF0000u, v[1].x & 0xFFFF0000u, v[2].x & 0xFFFF0000u, v[3].x & 0xFFFF0000u}, ro, o + 2 * DM * 4, 0, OUT_AUX);
        __builtin_amdgcn_raw_buffer_store_b128((u4){v[0].y << 16, v[1].y << 16, v[2].y << 16, v[3].y << 16}, ro, o + 4 * DM * 4, 0, OUT_AUX);
        __builtin_amdgcn_raw_buffer_store_b128((u4){v[0].y & 0xFFFF0000u, v[1].y & 0xFFFF0000u, v[2].y & 0xFFFF0000u, v[3].y & 0xFFFF0000u}, ro, o + 6 * DM * 4, 0, OUT_AUX);
    }
    STAMP(5);
    PROBE_END;
}

__global__ __launch_bounds__(512, 2) void kAll(Args a) {
    extern __shared__ __attribute__((aligned(16))) unsigned char smem[];
    LAS unsigned char* lds = (LAS unsigned char*)smem;
    const int b = blockIdx.x, xcd = b & 7, i = b >> 3;
    const int lane = threadIdx.x & 63, w = __builtin_amdgcn_readfirstlane(threadIdx.x >> 6);
    if (i < 18) {
        const int c = 2 * xcd + (i >= 9), g = i % 9;
        if (w < 4) b_compute(a, c, g, w, lane, lds);
        else b_loader(a, c, g, w - 4, lane, lds);
    } else a_block(a, 9 * xcd + (i - 18), w, lane, lds);
}

extern "C" void kernel_launch(void* const* d_in, const int* in_sizes, int n_in, void* d_out, int out_size, void* d_ws, size_t ws_size, hipStream_t stream) {
    Args a;
    a.u = (const u16*)d_in[0]; a.logA = (const u16*)d_in[1]; a.Aim = (const u16*)d_in[2]; a.CreA = (const u16*)d_in[3]; a.CimA = (const u16*)d_in[4];
    a.logdtA = (const u16*)d_in[5]; a.DskA = (const u16*)d_in[6];
    a.x = (const u16*)d_in[7]; a.Lre = (const u16*)d_in[8]; a.Lim = (const u16*)d_in[9]; a.Bre = (const u16*)d_in[10]; a.Bim = (const u16*)d_in[11];
    a.Cre = (const u16*)d_in[12]; a.Cim = (const u16*)d_in[13]; a.logdtB = (const u16*)d_in[14]; a.DskB = (const u16*)d_in[15];
    a.out = (float*)d_out; a.ws = (unsigned char*)d_ws;
    (void)hipFuncSetAttribute((const void*)kAll, hipFuncAttributeMaxDynamicSharedMemorySize, LDS_BYTES);
    kAll<<<216, 512, LDS_BYTES, stream>>>(a);
}
```

```cpp
#include <hip/hip_runtime.h>
#include <stdint.h>
#include <type_traits>
#define GAS __attribute__((address_space(1)))
#define LAS __attribute__((address_space(3)))
typedef unsigned short u16; typedef unsigned u32; typedef unsigned long long u64;
typedef float f2 __attribute__((ext_vector_type(2)));
typedef float f4 __attribute__((ext_vector_type(4)));
typedef float f16v __attribute__((ext_vector_type(16)));
typedef short bf8 __attribute__((ext_vector_type(8)));
typedef u32 u4 __attribute__((ext_vector_type(4)));
typedef u32 u2 __attribute__((ext_vector_type(2)));
typedef u32 u16v __attribute__((ext_vector_type(16)));
typedef __bf16 b2v __attribute__((ext_vector_type(2)));
typedef const GAS unsigned char* gptr;

#define OUT_AUX_A 0
#define OUT_AUX_B 0
constexpr int SEQ = 2048, DM = 576, NS = 128;
constexpr size_t WS_E    = 0;
constexpr size_t WS_FLAG = 4096;
constexpr size_t WS_AGG  = 65536;
constexpr size_t WS_H    = 1u << 20;
constexpr int LDS_BYTES = 160 * 1024;
constexpr int SPIN_CAP = 100000;
#define RLX_AGENT __ATOMIC_RELAXED, __HIP_MEMORY_SCOPE_AGENT

struct Args {
    const u16 *u, *logA, *Aim, *CreA, *CimA, *logdtA, *DskA;
    const u16 *x, *Lre, *Lim, *Bre, *Bim, *Cre, *Cim, *logdtB, *DskB;
    float* out; unsigned char* ws;
};
struct KA { u16v k0, k1; u4 k2; };
#define KPTR(v, i) ((gptr)(((u64)(v)[2 * (i) + 1] << 32) | (v)[2 * (i)]))
#define P_U(ka)      KPTR((ka).k0, 0)
#define P_LOGA(ka)   KPTR((ka).k0, 1)
#define P_AIM(ka)    KPTR((ka).k0, 2)
#define P_CREA(ka)   KPTR((ka).k0, 3)
#define P_CIMA(ka)   KPTR((ka).k0, 4)
#define P_LOGDTA(ka) KPTR((ka).k0, 5)
#define P_DSKA(ka)   KPTR((ka).k0, 6)
#define P_X(ka)      KPTR((ka).k0, 7)
#define P_LRE(ka)    KPTR((ka).k1, 0)
#define P_LIM(ka)    KPTR((ka).k1, 1)
#define P_BRE(ka)    KPTR((ka).k1, 2)
#define P_BIM(ka)    KPTR((ka).k1, 3)
#define P_CRE(ka)    KPTR((ka).k1, 4)
#define P_CIM(ka)    KPTR((ka).k1, 5)
#define P_LOGDTB(ka) KPTR((ka).k1, 6)
#define P_DSKB(ka)   KPTR((ka).k1, 7)
#define P_OUT(ka)    KPTR((ka).k2, 0)
#define P_WS(ka)     KPTR((ka).k2, 1)

__device__ __forceinline__ float bf2f(u32 v) { return __uint_as_float(v << 16); }
__device__ __forceinline__ float bflo(u32 v) { return __uint_as_float(v << 16); }
__device__ __forceinline__ float bfhi(u32 v) { return __uint_as_float(v & 0xFFFF0000u); }
__device__ __forceinline__ u32 cvtpk(float lo, float hi) { f2 v = {lo, hi}; return __builtin_bit_cast(u32, __builtin_convertvector(v, b2v)); }
__device__ __forceinline__ float rbf(float f) { return __uint_as_float(cvtpk(f, f) << 16); }
__device__ __forceinline__ float fexp(float x) { return __builtin_amdgcn_exp2f(x * 1.4426950408889634f); }
__device__ __forceinline__ void bar() { asm volatile("s_barrier" ::: "memory"); }
#define WAIT_LGKM0() __builtin_amdgcn_s_waitcnt(0xC07F)
template <int CTRL> __device__ __forceinline__ float dpp(float v) {
    return __builtin_bit_cast(float, __builtin_amdgcn_update_dpp(0, __builtin_bit_cast(int, v), CTRL, 0xf, 0xf, true));
}
template <int CTRL> __device__ __forceinline__ f2 dpp2(f2 v) { return (f2){dpp<CTRL>(v.x), dpp<CTRL>(v.y)}; }
__device__ __forceinline__ f2 allred16(f2 v) {
    v += dpp2<0x128>(v); v += dpp2<0x124>(v); v += dpp2<0x122>(v); v += dpp2<0x121>(v); return v;
}
#define FMA2(a, b, c) __builtin_elementwise_fma((a), (b), (c))
struct c2 { f2 r, i; };
__device__ __forceinline__ c2 cmul(c2 a, c2 b) { c2 o; o.r = FMA2(a.r, b.r, -(a.i * b.i)); o.i = FMA2(a.r, b.i, a.i * b.r); return o; }
__device__ __forceinline__ c2 cfma(c2 a, c2 b, c2 c) { c2 o; o.r = FMA2(a.r, b.r, FMA2(-a.i, b.i, c.r)); o.i = FMA2(a.r, b.i, FMA2(a.i, b.r, c.i)); return o; }
__device__ __forceinline__ void cpowf(float k, float ar2, float ai2, float& re, float& im) {
    const float e = __builtin_amdgcn_exp2f(k * ar2);
    const float t = __builtin_amdgcn_fractf(k * ai2);
    re = e * __builtin_amdgcn_cosf(t); im = e * __builtin_amdgcn_sinf(t);
}
__device__ __forceinline__ c2 cpow2(float k, f2 ar2, f2 ai2) {
    float r0, i0, r1, i1; cpowf(k, ar2.x, ai2.x, r0, i0); cpowf(k, ar2.y, ai2.y, r1, i1);
    c2 o; o.r = (f2){r0, r1}; o.i = (f2){i0, i1}; return o;
}
#define PIN2(v) asm volatile("" : "+v"((v).r), "+v"((v).i))
__device__ __forceinline__ __amdgpu_buffer_rsrc_t mkrsrc(gptr p, u32 bytes) { return __builtin_amdgcn_make_buffer_rsrc((GAS void*)p, 0, bytes, 0x00020000); }
template <int GO, int LO> __device__ __forceinline__ void glds16(u32 voff, gptr sbase, u32 slds) {
    u32 t;
    asm volatile("s_add_u32 m0, %3, %5\n\tv_add_u32 %0, %4, %1\n\tglobal_load_lds_dwordx4 %0, %2"
                 : "=&v"(t) : "v"(voff), "s"(sbase), "s"(slds), "i"(GO), "i"(LO) : "memory", "scc");
}
template <int I, int N, class F> __device__ __forceinline__ void sfor(F&& f) { if constexpr (I < N) { f(std::integral_constant<int, I>{}); sfor<I + 1, N>(f); } }

constexpr int C_KC = 16384, C_COMP = 8192, LDS_MISC = 9 * C_KC;

__device__ __forceinline__ void b_loader(KA ka, int c, int g, int lw, int lane, LAS unsigned char* lds) {
    const gptr ws = P_WS(ka);
    u64 ev;
    asm volatile("global_load_dwordx2 %0, %1, %2 sc1" : "=v"(ev) : "v"(0u), "s"(ws + WS_E) : "memory");
    const int r8 = lane >> 3, sl = lane & 7;
    const int rw = 8 * lw + r8;
    const u32 swz = (u32)((sl ^ ((rw >> 1) & 7)) << 4);
    const u32 vW = (u32)(g * 64 + (rw & 15)) * 1152 + swz;
    const u32 vX = (u32)(c * 128 + 8 * (rw & 15) + (rw >> 4)) * 1152 + swz;
    const gptr bW = ((lw >> 1) & 1) ? P_BIM(ka) : P_BRE(ka), bX = P_X(ka);
    const u32 sl_ = (u32)lw * 1024;
    auto issue = [&](auto S) {
        constexpr int s = S.value;
        sfor<0, 4>([&](auto J) { constexpr int j = J.value; glds16<s * 128 + j * (16 * 1152), (s & 3) * 32768 + j * 4096>(vW, bW, sl_); });
        sfor<0, 4>([&](auto J) { constexpr int j = J.value; glds16<s * 128 + j * (2 * 1152), (s & 3) * 32768 + 16384 + j * 4096>(vX, bX, sl_); });
    };
    const int rc = 8 * lw + r8;
    const u32 vC = (u32)(g * 64 + rc) * 1152 + (u32)((sl ^ ((rc >> 1) & 7)) << 4);
    const gptr bCre = P_CRE(ka), bCim = P_CIM(ka);
    auto load_c = [&](auto KC) {
        constexpr int kc = KC.value;
        sfor<0, 4>([&](auto I) { constexpr int i = I.value; glds16<kc * 128 + (i & 1) * (32 * 1152), kc * C_KC + i * 4096>(vC, (i >> 1) ? bCim : bCre, sl_); });
    };
#define IC(v) std::integral_constant<int, v>{}
    issue(IC(0)); issue(IC(1));
    asm volatile("s_waitcnt vmcnt(8)" : "+v"(ev) :: "memory");
    const u64 tag = ev + 1;
    if (lw == 0 && lane == 0) *(LAS u64*)(lds + LDS_MISC) = tag;
    asm volatile("s_waitcnt lgkmcnt(0)" ::: "memory");
    bar();
    issue(IC(2)); issue(IC(3));
    sfor<0, 9>([&](auto S) {
        constexpr int s = S.value;
        if constexpr (s <= 4) asm volatile("s_waitcnt vmcnt(16)" ::: "memory");
        else if constexpr (s <= 7) asm volatile("s_waitcnt vmcnt(20)" ::: "memory");
        bar();
        if constexpr (s + 4 <= 8) issue(IC(s + 4 <= 8 ? s + 4 : 8));
        if constexpr (s == 4) load_c(IC(8));
        if constexpr (s == 5) { load_c(IC(2)); load_c(IC(3)); }
        if constexpr (s == 6) { load_c(IC(4)); load_c(IC(5)); }
        if constexpr (s == 7) { load_c(IC(6)); load_c(IC(7)); }
        if constexpr (s == 8) { load_c(IC(0)); load_c(IC(1)); }
    });
    bar();
    if (lw == 0) {
        const GAS u64* fl = (const GAS u64*)(ws + WS_FLAG + (size_t)(c * 9 + (lane < 9 ? lane : 0)) * 128);
        for (int spin = 0; spin < SPIN_CAP; spin++) {
            const u64 v = __hip_atomic_load(fl, RLX_AGENT);
            if (__all(v == tag)) break;
            __builtin_amdgcn_s_sleep(1);
        }
        if (c == 15 && g == 0 && lane == 0) __hip_atomic_store((GAS u64*)(ws + WS_E), tag, RLX_AGENT);
    }
    asm volatile("s_waitcnt vmcnt(0)" ::: "memory");
    bar();
}

constexpr int WIN = 18;
__device__ __forceinline__ void b_compute(KA ka, int c, int g, int w, int lane, LAS unsigned char* lds) {
    const int n = lane & 15, q = lane >> 4;
    const int pw0 = g * 64 + 16 * w + 4 * q;
    const gptr ws = P_WS(ka);
    const int pmy = pw0 + (n & 3);
    const u32 r_lr = ((const GAS u16*)P_LRE(ka))[pmy], r_li = ((const GAS u16*)P_LIM(ka))[pmy], r_dt = ((const GAS u16*)P_LOGDTB(ka))[pmy];
    f4 acc[8][2];
#pragma unroll
    for (int t = 0; t < 8; t++) { acc[t][0] = (f4){0, 0, 0, 0}; acc[t][1] = (f4){0, 0, 0, 0}; }
    const u32 sw = (u32)(n * 128 + ((q ^ ((n >> 1) & 7)) << 4));
    const u32 aW = sw + w * 4096, aX = sw + 16384;
    bf8 fw[2][2], fx[2][8];
    auto rd = [&](int buf, u32 slotoff, int ks) {
        const u32 w_ = (aW ^ (ks << 6)) + slotoff, x_ = (aX ^ (ks << 6)) + slotoff;
        fw[buf][0] = *(const LAS bf8*)(lds + w_); fw[buf][1] = *(const LAS bf8*)(lds + w_ + 2048);
#pragma unroll
        for (int t = 0; t < 8; t++) fx[buf][t] = *(const LAS bf8*)(lds + x_ + t * 2048);
    };
    auto mm = [&](int buf) {
#pragma unroll
        for (int t = 0; t < 8; t++) {
            acc[t][0] = __builtin_amdgcn_mfma_f32_16x16x32_bf16(fw[buf][0], fx[buf][t], acc[t][0], 0, 0, 0);
            acc[t][1] = __builtin_amdgcn_mfma_f32_16x16x32_bf16(fw[buf][1], fx[buf][t], acc[t][1], 0, 0, 0);
        }
    };
    f2 ar2[2], ai2[2]; c2 Lam[2], coef[2], W1[2];
    auto params = [&]() {
        const float lr = bf2f(r_lr), li = bf2f(r_li);
        const float dt = rbf(fexp(bf2f(r_dt)));
        const float a2 = (dt * lr) * 1.4426950408889634f, b2 = (dt * li) * 0.15915494309189535f;
        float Lr, Li; cpowf(1.f, a2, b2, Lr, Li);
        const float nr = Lr - 1.f, ni = Li, den = __builtin_amdgcn_rcpf(lr * lr + li * li);
        const float cr = (nr * lr + ni * li) * den, ci = (ni * lr - nr * li) * den;
#define QB(dst, v) { dst[0] = (f2){dpp<0x00>(v), dpp<0x55>(v)}; dst[1] = (f2){dpp<0xAA>(v), dpp<0xFF>(v)}; }
        f2 t_[2];
        QB(ar2, a2); QB(ai2, b2);
        QB(t_, Lr); Lam[0].r = t_[0]; Lam[1].r = t_[1]; QB(t_, Li); Lam[0].i = t_[0]; Lam[1].i = t_[1];
        QB(t_, cr); coef[0].r = t_[0]; coef[1].r = t_[1]; QB(t_, ci); coef[0].i = t_[0]; coef[1].i = t_[1];
        W1[0] = cpow2((float)(8 * (15 - n)), ar2[0], ai2[0]); W1[1] = cpow2((float)(8 * (15 - n)), ar2[1], ai2[1]);
    };
    bar();
    const u64 tag = *(const LAS u64*)(lds + LDS_MISC);
    rd(0, 0, 0);
    WAIT_LGKM0();
#define ILV() { _Pragma("unroll") for (int i_ = 0; i_ < 10; i_++) { __builtin_amdgcn_sched_group_barrier(0x8, 1, 0); __builtin_amdgcn_sched_group_barrier(0x100, 1, 0); } __builtin_amdgcn_sched_group_barrier(0x8, 6, 0); }
#pragma unroll
    for (int s = 0; s < 9; s++) {
        __builtin_amdgcn_sched_barrier(0);
        rd(1, (s & 3) * 32768, 1);
        mm(0);
        ILV();
        __builtin_amdgcn_sched_barrier(0);
        WAIT_LGKM0();
        bar();
        __builtin_amdgcn_sched_barrier(0);
        if (s < 8) rd(0, ((s + 1) & 3) * 32768, 0);
        if (s == 2) params();
        mm(1);
        if (s < 8) ILV();
        __builtin_amdgcn_sched_barrier(0);
        WAIT_LGKM0();
    }
    const u32 tlo = (u32)tag, thi = (u32)(tag >> 32) & 0xFFu;
    c2 z[8][2];
#pragma unroll
    for (int t = 0; t < 8; t++) {
        z[t][0].r = acc[t][0].xy; z[t][1].r = acc[t][0].zw; z[t][0].i = acc[t][1].xy; z[t][1].i = acc[t][1].zw;
    }
    c2 agg[2];
    agg[0] = z[0][0]; agg[1] = z[0][1];
#pragma unroll
    for (int t = 1; t < 8; t++) { agg[0] = cfma(Lam[0], agg[0], z[t][0]); agg[1] = cfma(Lam[1], agg[1], z[t][1]); }
    c2 S[2];
#pragma unroll
    for (int h = 0; h < 2; h++) { S[h] = cmul(W1[h], agg[h]); S[h].r = allred16(S[h].r); S[h].i = allred16(S[h].i); }
    const __amdgpu_buffer_rsrc_t rs = mkrsrc(ws, 0x7fffffff);
    {
        const float sr = (n & 2) ? ((n & 1) ? S[1].r.y : S[1].r.x) : ((n & 1) ? S[0].r.y : S[0].r.x);
        const float si = (n & 2) ? ((n & 1) ? S[1].i.y : S[1].i.x) : ((n & 1) ? S[0].i.y : S[0].i.x);
        u4 gr; gr.x = tlo; gr.y = ((__float_as_uint(sr) + 0x80u) & 0xFFFFFF00u) | thi;
        gr.z = tlo; gr.w = ((__float_as_uint(si) + 0x80u) & 0xFFFFFF00u) | thi;
        if (n < 4) __builtin_amdgcn_raw_buffer_store_b128(gr, rs, (u32)(WS_AGG + ((size_t)c * DM + pw0 + n) * 16), 0, 16);
    }
    __builtin_amdgcn_sched_barrier(0);
    const int t0 = c * 128;
    const int col = lane & 31, hh = lane >> 5;
    u32 xv[2][16]; u32 dsk[2];
    {
        const GAS u16* xp = (const GAS u16*)P_X(ka) + (size_t)(t0 + 32 * hh + 2 * w) * DM + g * 64 + col;
#pragma unroll
        for (int nt = 0; nt < 2; nt++) {
            dsk[nt] = ((const GAS u16*)P_DSKB(ka))[g * 64 + nt * 32 + col];
#pragma unroll
            for (int r = 0; r < 16; r++) xv[nt][r] = xp[(8 * (r & 3) + 64 * ((r >> 2) & 1) + (r >> 3)) * DM + nt * 32];
        }
    }
    c2 E[2], W2[2], PW[2];
#pragma unroll
    for (int h = 0; h < 2; h++) {
        c2 P = agg[h];
        c2 m = cpow2(8.f, ar2[h], ai2[h]);
        { c2 s; s.r = dpp2<0x111>(P.r); s.i = dpp2<0x111>(P.i); P = cfma(m, s, P); } m = cmul(m, m);
        { c2 s; s.r = dpp2<0x112>(P.r); s.i = dpp2<0x112>(P.i); P = cfma(m, s, P); } m = cmul(m, m);
        { c2 s; s.r = dpp2<0x114>(P.r); s.i = dpp2<0x114>(P.i); P = cfma(m, s, P); } m = cmul(m, m);
        { c2 s; s.r = dpp2<0x118>(P.r); s.i = dpp2<0x118>(P.i); P = cfma(m, s, P); }
        E[h].r = dpp2<0x111>(P.r); E[h].i = dpp2<0x111>(P.i);
        W2[h] = cpow2((float)(128 * (15 - n)), ar2[h], ai2[h]);
        PW[h] = cmul(coef[h], cpow2((float)(8 * n), ar2[h], ai2[h]));
        E[h] = cmul(coef[h], E[h]);
#pragma unroll
        for (int t = 0; t < 8; t++) { z[t][h] = cmul(coef[h], z[t][h]); PIN2(z[t][h]); }
        PIN2(E[h]); PIN2(W2[h]); PIN2(PW[h]);
    }
    u32 skp[2][8];
#pragma unroll
    for (int nt = 0; nt < 2; nt++) {
        const float d = bf2f(dsk[nt]);
#pragma unroll
        for (int r = 0; r < 8; r++) { skp[nt][r] = cvtpk(bf2f(xv[nt][2 * r]) * d, bf2f(xv[nt][2 * r + 1]) * d); asm volatile("" : "+v"(skp[nt][r])); }
    }
    c2 X[2];
    {
        const int cp = c - 16 + n;
        const bool valid = cp >= 0;
        const u32 off = (u32)(WS_AGG + ((size_t)(valid ? cp : 0) * DM + pw0) * 16);
        u4 g0, g1, g2, g3;
        for (int spin = 0; spin < SPIN_CAP; spin++) {
            g0 = __builtin_amdgcn_raw_buffer_load_b128(rs, off, 0, 16);
            g1 = __builtin_amdgcn_raw_buffer_load_b128(rs, off + 16, 0, 16);
            g2 = __builtin_amdgcn_raw_buffer_load_b128(rs, off + 32, 0, 16);
            g3 = __builtin_amdgcn_raw_buffer_load_b128(rs, off + 48, 0, 16);
            const u32 bad = (g0.x ^ tlo) | (g0.z ^ tlo) | (g1.x ^ tlo) | (g1.z ^ tlo) | (g2.x ^ tlo) | (g2.z ^ tlo) | (g3.x ^ tlo) | (g3.z ^ tlo)
                | (((g0.y ^ thi) | (g0.w ^ thi) | (g1.y ^ thi) | (g1.w ^ thi) | (g2.y ^ thi) | (g2.w ^ thi) | (g3.y ^ thi) | (g3.w ^ thi)) & 0xFFu);
            if (__all(!valid || bad == 0)) break;
            __builtin_amdgcn_s_sleep(1);
        }
        const u32 msk = valid ? 0xFFFFFF00u : 0u;
        c2 Sp[2];
        Sp[0].r = (f2){__uint_as_float(g0.y & msk), __uint_as_float(g1.y & msk)}; Sp[0].i = (f2){__uint_as_float(g0.w & msk), __uint_as_float(g1.w & msk)};
        Sp[1].r = (f2){__uint_as_float(g2.y & msk), __uint_as_float(g3.y & msk)}; Sp[1].i = (f2){__uint_as_float(g2.w & msk), __uint_as_float(g3.w & msk)};
#pragma unroll
        for (int h = 0; h < 2; h++) { X[h] = cmul(W2[h], Sp[h]); X[h].r = allred16(X[h].r); X[h].i = allred16(X[h].i); }
    }
    c2 hst[2];
#pragma unroll
    for (int h = 0; h < 2; h++) hst[h] = cfma(PW[h], X[h], E[h]);
    const u32 hoff = (u32)(WS_H + ((size_t)(c * 36 + g * 4 + w) * 8) * 1024 + (q & 1) * 512 + (q >> 1) * 256 + n * 16);
#pragma unroll
    for (int t = 0; t < 8; t++) {
        hst[0] = cfma(Lam[0], hst[0], z[t][0]); hst[1] = cfma(Lam[1], hst[1], z[t][1]);
        const auto s0 = __builtin_amdgcn_permlane16_swap(cvtpk(hst[0].r.x, hst[0].r.y), cvtpk(-hst[0].i.x, -hst[0].i.y), false, false);
        const auto s1 = __builtin_amdgcn_permlane16_swap(cvtpk(hst[1].r.x, hst[1].r.y), cvtpk(-hst[1].i.x, -hst[1].i.y), false, false);
        __builtin_amdgcn_raw_buffer_store_b128((u4){s0[0], s1[0], s0[1], s1[1]}, rs, hoff + t * 1024, 0, 16);
    }
    asm volatile("s_waitcnt vmcnt(0)" ::: "memory");
    bar();
    if (w == 0 && lane == 0) __hip_atomic_store((GAS u64*)(ws + WS_FLAG + (size_t)(c * 9 + g) * 128), tag, RLX_AGENT);
    const u32 hb = (u32)(WS_H + ((size_t)c * 36 * 8 + 2 * w) * 1024 + (col >> 4) * 1024 + hh * 256 + (col & 15) * 16);
    u4 fa[WIN][2];
    auto kcof = [&](int i) { int k = g + i; return k >= 9 ? k - 9 : k; };
    auto ldh = [&](int ci) {
        const u32 so = (u32)kcof(ci >> 2) * 32768u;
        fa[ci % WIN][0] = __builtin_amdgcn_raw_buffer_load_b128(rs, hb + (ci & 3) * 8192, so, 16);
        fa[ci % WIN][1] = __builtin_amdgcn_raw_buffer_load_b128(rs, hb + (ci & 3) * 8192 + 512, so, 16);
    };
#pragma unroll
    for (int k = 0; k < 4; k++) ldh(k);
    bar();
#pragma unroll
    for (int k = 4; k < WIN; k++) ldh(k);
    f16v ac2[2];
#pragma unroll
    for (int nt = 0; nt < 2; nt++)
#pragma unroll
        for (int r = 0; r < 16; r++) ac2[nt][r] = 0.f;
    u32 ab[4];
#pragma unroll
    for (int cc = 0; cc < 4; cc++) ab[cc] = col * 128 + (((2 * cc + hh) ^ ((col >> 1) & 7)) << 4);
    bf8 fb[2][4];
    auto rdc = [&](int ci) {
        const u32 ad = ab[ci & 3] + (u32)kcof(ci >> 2) * C_KC;
#pragma unroll
        for (int j = 0; j < 4; j++) fb[ci & 1][j] = *(const LAS bf8*)(lds + ad + (j & 1) * 4096 + (j >> 1) * C_COMP);
    };
    rdc(0);
#pragma unroll
    for (int ci = 0; ci < 36; ci++) {
        __builtin_amdgcn_sched_barrier(0);
        if (ci + 1 < 36) rdc(ci + 1);
        const bf8 a0 = __builtin_bit_cast(bf8, fa[ci % WIN][0]), a1 = __builtin_bit_cast(bf8, fa[ci % WIN][1]);
        ac2[0] = __builtin_amdgcn_mfma_f32_32x32x16_bf16(a0, fb[ci & 1][0], ac2[0], 0, 0, 0);
        ac2[1] = __builtin_amdgcn_mfma_f32_32x32x16_bf16(a0, fb[ci & 1][1], ac2[1], 0, 0, 0);
        ac2[0] = __builtin_amdgcn_mfma_f32_32x32x16_bf16(a1, fb[ci & 1][2], ac2[0], 0, 0, 0);
        ac2[1] = __builtin_amdgcn_mfma_f32_32x32x16_bf16(a1, fb[ci & 1][3], ac2[1], 0, 0, 0);
        if (ci + 1 < 36) { _Pragma("unroll") for (int i_ = 0; i_ < 4; i_++) { __builtin_amdgcn_sched_group_barrier(0x8, 1, 0); __builtin_amdgcn_sched_group_barrier(0x100, 1, 0); } }
        __builtin_amdgcn_sched_barrier(0);
        if (ci + WIN < 36) ldh(ci + WIN);
    }
    const __amdgpu_buffer_rsrc_t ro = mkrsrc(P_OUT(ka), SEQ * 2 * DM * 4);
    const u32 ob = (u32)(((t0 + 32 * hh + 2 * w) * (2 * DM) + DM + g * 64 + col) * 4);
#pragma unroll
    for (int r = 0; r < 16; r++)
#pragma unroll
        for (int nt = 0; nt < 2; nt++) {
            const u32 sk = skp[nt][r >> 1];
            const float v = 2.f * ac2[nt][r] + ((r & 1) ? bfhi(sk) : bflo(sk));
            __builtin_amdgcn_raw_buffer_store_b32(__float_as_uint(v), ro, ob + nt * 128, (8 * (r & 3) + 64 * ((r >> 2) & 1) + (r >> 3)) * (2 * DM * 4), OUT_AUX_B);
        }
}

constexpr int UT_STRIDE = 4640;
constexpr int A_KR = 8 * UT_STRIDE;
constexpr int KR_BYTES = 8704, KV_BYTES = 1344, KR_B = 4160, TROW = 544;
constexpr int A_WAVE = KR_BYTES + KV_BYTES;

__device__ __forceinline__ u32 pk16(u32 lo, u32 hi) { return __builtin_amdgcn_perm(hi, lo, 0x05040100u); }

__device__ __forceinline__ void a_block(KA ka, int ga, int w, int lane, LAS unsigned char* lds) {
    const int d0 = ga * 8, d = d0 + w, tid = w * 64 + lane;
    const u32 pA = *(const GAS u32*)(P_LOGA(ka) + (d * NS + 2 * lane) * 2), pI = *(const GAS u32*)(P_AIM(ka) + (d * NS + 2 * lane) * 2);
    const u32 pR = *(const GAS u32*)(P_CREA(ka) + (d * NS + 2 * lane) * 2), pC = *(const GAS u32*)(P_CIMA(ka) + (d * NS + 2 * lane) * 2);
    const u32 r_dt = ((const GAS u16*)P_LOGDTA(ka))[d], r_dk = ((const GAS u16*)P_DSKA(ka))[d];
    u4 ur[4];
#pragma unroll
    for (int i = 0; i < 4; i++) ur[i] = *(const GAS u4*)(P_U(ka) + ((size_t)(4 * tid + i) * DM + d0) * 2);
    LAS unsigned char* kr = lds + A_KR + w * A_WAVE;
    LAS unsigned char* kv = kr + KR_BYTES;
    const float dt = rbf(fexp(bf2f(r_dt))), dsk = bf2f(r_dk);
    const f2 are = {-rbf(fexp(bflo(pA))) * dt, -rbf(fexp(bfhi(pA))) * dt};
    const f2 aim = (f2){bflo(pI), bfhi(pI)} * dt;
    const f2 a2 = are * 1.4426950408889634f, b2 = aim * 0.15915494309189535f;
    c2 Cc; Cc.r = (f2){bflo(pR), bfhi(pR)} * dt * 2.f; Cc.i = (f2){bflo(pC), bfhi(pC)} * dt * 2.f;
    const c2 w1 = cpow2(1.f, a2, b2), w8 = cpow2(8.f, a2, b2), w128 = cpow2(128.f, a2, b2);
    const c2 w2 = cmul(w1, w1), w4 = cmul(w2, w2);
    auto put = [&](int row, c2 v) { *(LAS u2*)(kr + row * TROW + lane * 8) = (u2){cvtpk(v.r.x, v.i.x), cvtpk(v.r.y, v.i.y)}; };
    const int jx = lane & 15, g4 = lane >> 4;
    const u32 fra = jx * TROW + g4 * 16;
    bf8 fr[8], fl[8];
    {
        c2 v; v.r = (f2){1.f, 1.f}; v.i = (f2){0.f, 0.f};
#pragma unroll
        for (int j = 0; j < 16; j++) { c2 t; t.r = v.r; t.i = -v.i; put(j, t); if (j < 15) v = cmul(v, w8); }
#pragma unroll
        for (int s = 0; s < 8; s++) fr[s] = *(const LAS bf8*)(kr + fra + s * 64);
    }
    auto rdl = [&]() {
#pragma unroll
        for (int s = 0; s < 8; s++) fl[s] = *(const LAS bf8*)(kr + fra + s * 64);
        __builtin_amdgcn_sched_barrier(0);
    };
    auto tile = [&]() {
        f4 a0 = {0, 0, 0, 0}, a1 = {0, 0, 0, 0};
#pragma unroll
        for (int s = 0; s < 8; s += 2) {
            a0 = __builtin_amdgcn_mfma_f32_16x16x32_bf16(fl[s], fr[s], a0, 0, 0, 0);
            a1 = __builtin_amdgcn_mfma_f32_16x16x32_bf16(fl[s + 1], fr[s + 1], a1, 0, 0, 0);
        }
        return a0 + a1;
    };
    c2 G = Cc;
#pragma unroll
    for (int i = 0; i < 2; i++) {
        c2 v = G;
#pragma unroll
        for (int e = 0; e < 8; e++) { put(8 * i + e, v); if (e < 7) v = cmul(v, w1); }
        G = cmul(G, w128);
    }
    rdl();
    const f4 kt0 = tile();
#pragma unroll
    for (int i = 0; i < 2; i++) {
        c2 v = G;
#pragma unroll
        for (int e = 0; e < 4; e++) { put(4 * i + e, v); if (e < 3) v = cmul(v, w2); }
        G = cmul(G, w128);
    }
#pragma unroll
    for (int i = 0; i < 4; i++) { put(8 + 2 * i, G); put(9 + 2 * i, cmul(G, w4)); G = cmul(G, w128); }
    rdl();
    const f4 kt1 = tile();
#pragma unroll
    for (int i = 0; i < 9; i++) { put(i, G); if (i < 8) G = cmul(G, w128); }
    rdl();
    const f4 kt2 = tile();
    {
        *(LAS u2*)(kv + 2 * (128 * (g4 >> 1) + 8 * jx + 4 * (g4 & 1))) = (u2){cvtpk(kt0[0], kt0[1]), cvtpk(kt0[2], kt0[3])};
        const int i0 = g4 < 2 ? 256 + 64 * g4 + 4 * jx : 384 + 64 * (g4 - 2) + 2 * jx, i1 = i0 + (g4 < 2 ? 2 : 32);
        *(LAS u32*)(kv + 2 * i0) = cvtpk(kt1[0], kt1[1]); *(LAS u32*)(kv + 2 * i1) = cvtpk(kt1[2], kt1[3]);
        const u32 k01 = cvtpk(kt2[0], kt2[1]), k23 = cvtpk(kt2[2], kt2[3]);
        if (g4 < 2) {
            LAS u16* p = (LAS u16*)(kv + 2 * (512 + 64 * g4 + jx));
            p[0] = (u16)k01; p[16] = (u16)(k01 >> 16); p[32] = (u16)k23; p[48] = (u16)(k23 >> 16);
        } else if (lane == 32) *(LAS u16*)(kv + 2 * 640) = (u16)k01;
    }
    {
        const LAS u16* kvp = (const LAS u16*)kv;
        u32 k[4][8];
        {
            const bool s1 = lane >= 32;
            const int base = s1 ? 256 + 4 * (lane - 32) : 8 * lane;
            const int o1[8] = {0, 0, 1, 2, 2, 2, 3, 4};
#pragma unroll
            for (int i = 0; i < 8; i++) k[0][i] = kvp[base + (s1 ? o1[i] : i)];
        }
        {
            const int base = 384 + 2 * lane;
            const u32 v0 = kvp[base], v1 = kvp[base + 1], v2 = kvp[base + 2];
            k[1][0] = k[1][1] = k[1][2] = v0; k[1][3] = k[1][4] = k[1][5] = v1; k[1][6] = k[1][7] = v2;
        }
#pragma unroll
        for (int qq = 2; qq < 4; qq++) {
            const int base = 512 + 64 * (qq - 2) + lane;
            const u32 v0 = kvp[base], v1 = kvp[base + 1];
            k[qq][0] = k[qq][1] = k[qq][2] = k[qq][3] = v0; k[qq][4] = (lane & 1) ? v1 : v0; k[qq][5] = k[qq][6] = k[qq][7] = v1;
        }
#pragma unroll
        for (int qq = 0; qq < 4; qq++) {
            const int c = 64 * qq + lane;
            const u32 prev7 = qq ? (u32)__builtin_amdgcn_readlane((int)k[qq ? qq - 1 : 0][7], 63) : 0u;
            const u32 km1 = (u32)__builtin_amdgcn_update_dpp((int)prev7, (int)k[qq][7], 0x138, 0xf, 0xf, false);
            *(LAS u4*)(kr + 16 * (255 - c)) = (u4){pk16(k[qq][7], k[qq][6]), pk16(k[qq][5], k[qq][4]), pk16(k[qq][3], k[qq][2]), pk16(k[qq][1], k[qq][0])};
            *(LAS u4*)(kr + KR_B + 16 * (255 - c)) = (u4){pk16(k[qq][6], k[qq][5]), pk16(k[qq][4], k[qq][3]), pk16(k[qq][2], k[qq][1]), pk16(k[qq][0], km1)};
        }
        if (lane < 2) { *(LAS u4*)(kr + 4096 + lane * 16) = (u4){0, 0, 0, 0}; *(LAS u4*)(kr + KR_B + 4096 + lane * 16) = (u4){0, 0, 0, 0}; }
    }
#pragma unroll
    for (int j = 0; j < 4; j++) {
        const u32 r0 = ur[0][j], r1 = ur[1][j], r2 = ur[2][j], r3 = ur[3][j];
        *(LAS u2*)(lds + (2 * j) * UT_STRIDE + 512 + 8 * tid) = (u2){__builtin_amdgcn_perm(r1, r0, 0x05040100u), __builtin_amdgcn_perm(r3, r2, 0x05040100u)};
        *(LAS u2*)(lds + (2 * j + 1) * UT_STRIDE + 512 + 8 * tid) = (u2){__builtin_amdgcn_perm(r1, r0, 0x07060302u), __builtin_amdgcn_perm(r3, r2, 0x07060302u)};
    }
    if (lane < 32) *(LAS u4*)(lds + w * UT_STRIDE + lane * 16) = (u4){0, 0, 0, 0};
    asm volatile("s_waitcnt lgkmcnt(0)" ::: "memory");
    bar();
    f4 acc[8];
#pragma unroll
    for (int t = 0; t < 8; t++) acc[t] = (f4){0, 0, 0, 0};
    {
        const int b = jx, kk = g4 >> 1, eh = g4 & 1;
        u32 ka_ = (u32)(A_KR + w * A_WAVE) + ((b & 1) ? 2 * (2047 - 16 * kk + 8 * eh - b) : KR_B + 2 * (2046 - 16 * kk + 8 * eh - b)) - 3584;
        u32 ua = (u32)(w * UT_STRIDE) + 512 + 32 * jx - 32 * kk + 16 * eh;
        for (int r = 0; r < 8; r++) {
            bf8 fu[8];
#pragma unroll
            for (int t = 0; t < 8; t++) fu[t] = *(const LAS bf8*)(lds + ua + 512 * t);
#pragma unroll
            for (int i = 0; i < 8; i++) {
                const LAS u32* p = (const LAS u32*)(lds + ka_ + 512 * (7 - i));
                const bf8 fk = __builtin_bit_cast(bf8, (u4){p[0], p[1], p[2], p[3]});
#pragma unroll
                for (int t = i; t < 8; t++) acc[t] = __builtin_amdgcn_mfma_f32_16x16x32_bf16(fk, fu[t - i], acc[t], 0, 0, 0);
            }
            ka_ -= 64; ua -= 64;
        }
    }
#pragma unroll
    for (int t = 0; t < 8; t++) {
        LAS u2* p = (LAS u2*)(lds + w * UT_STRIDE + 512 + 2 * (256 * t + 16 * jx + 4 * g4));
        const u2 uu = *p;
        const u32 y01 = cvtpk(acc[t][0], acc[t][1]), y23 = cvtpk(acc[t][2], acc[t][3]);
        const u32 s01 = cvtpk(bflo(uu.x) * dsk, bfhi(uu.x) * dsk), s23 = cvtpk(bflo(uu.y) * dsk, bfhi(uu.y) * dsk);
        *p = (u2){cvtpk(bflo(y01) + bflo(s01), bfhi(y01) + bfhi(s01)), cvtpk(bflo(y23) + bflo(s23), bfhi(y23) + bfhi(s23))};
    }
    asm volatile("s_waitcnt lgkmcnt(0)" ::: "memory");
    bar();
    const int hf = tid & 1;
    const __amdgpu_buffer_rsrc_t ro = mkrsrc(P_OUT(ka), SEQ * 2 * DM * 4);
#pragma unroll
    for (int ps = 0; ps < 2; ps++) {
        const int rq = (tid >> 1) + 256 * ps;
        u2 v[4];
#pragma unroll
        for (int j = 0; j < 4; j++) v[j] = *(const LAS u2*)(lds + (4 * hf + j) * UT_STRIDE + 512 + 8 * rq);
        const u32 o = (u32)(((4 * rq) * (2 * DM) + d0 + 4 * hf) * 4);
        __builtin_amdgcn_raw_buffer_store_b128((u4){v[0].x << 16, v[1].x << 16, v[2].x << 16, v[3].x << 16}, ro, o, 0, OUT_AUX_A);
        __builtin_amdgcn_raw_buffer_store_b128((u4){v[0].x & 0xFFFF0000u, v[1].x & 0xFFFF0000u, v[2].x & 0xFFFF0000u, v[3].x & 0xFFFF0000u}, ro, o + 2 * DM * 4, 0, OUT_AUX_A);
        __builtin_amdgcn_raw_buffer_store_b128((u4){v[0].y << 16, v[1].y << 16, v[2].y << 16, v[3].y << 16}, ro, o + 4 * DM * 4, 0, OUT_AUX_A);
        __builtin_amdgcn_raw_buffer_store_b128((u4){v[0].y & 0xFFFF0000u, v[1].y & 0xFFFF0000u, v[2].y & 0xFFFF0000u, v[3].y & 0xFFFF0000u}, ro, o + 6 * DM * 4, 0, OUT_AUX_A);
    }
}

__device__ __forceinline__ void helper(KA ka, int xcd, int hw, int lane) {
    const int id = hw * 64 + lane;
    u32 t[13];
    t[0] = *(const GAS u32*)(P_X(ka) + (size_t)xcd * (256 * 1152) + min(id, 2303) * 128);
#pragma unroll
    for (int j = 0; j < 3; j++) {
        const int l = (j == 2 ? min(id + 5120, 5183) : id + 2560 * j) * 128;
        t[1 + 2 * j] = *(const GAS u32*)(P_BRE(ka) + l); t[2 + 2 * j] = *(const GAS u32*)(P_BIM(ka) + l);
    }
#pragma unroll
    for (int j = 0; j < 3; j++) {
        const int l = (j == 2 ? min(id + 5120, 5183) : id + 2560 * j) * 128;
        t[7 + 2 * j] = *(const GAS u32*)(P_CRE(ka) + l); t[8 + 2 * j] = *(const GAS u32*)(P_CIM(ka) + l);
    }
#pragma unroll
    for (int j = 0; j < 13; j++) asm volatile("" :: "v"(t[j]));
}

__global__ __launch_bounds__(512, 2) void kAll(Args a_unused) {
    extern __shared__ __attribute__((aligned(16))) unsigned char smem[];
    LAS unsigned char* lds = (LAS unsigned char*)smem;
    KA ka;
    asm volatile("s_load_dwordx16 %0, %3, 0x0\n\ts_load_dwordx16 %1, %3, 0x40\n\ts_load_dwordx4 %2, %3, 0x80\n\ts_waitcnt lgkmcnt(0)"
                 : "=&s"(ka.k0), "=&s"(ka.k1), "=&s"(ka.k2) : "s"(__builtin_amdgcn_kernarg_segment_ptr()));
    const int b = blockIdx.x, xcd = b & 7, i = b >> 3;
    const int lane = threadIdx.x & 63, w = __builtin_amdgcn_readfirstlane(threadIdx.x >> 6);
    if (i < 18) {
        const int c = 2 * xcd + (i >= 9), g = i % 9;
        if (w >= 4) b_loader(ka, c, g, w - 4, lane, lds);
        else b_compute(ka, c, g, w, lane, lds);
    } else if (i < 27) {
        a_block(ka, 9 * xcd + (i - 18), w, lane, lds);
    } else {
        helper(ka, xcd, (i - 27) * 8 + w, lane);
    }
}

extern "C" void kernel_launch(void* const* d_in, const int* in_sizes, int n_in, void* d_out, int out_size, void* d_ws, size_t ws_size, hipStream_t stream) {
    Args a;
    a.u = (const u16*)d_in[0]; a.logA = (const u16*)d_in[1]; a.Aim = (const u16*)d_in[2]; a.CreA = (const u16*)d_in[3]; a.CimA = (const u16*)d_in[4];
    a.logdtA = (const u16*)d_in[5]; a.DskA = (const u16*)d_in[6];
    a.x = (const u16*)d_in[7]; a.Lre = (const u16*)d_in[8]; a.Lim = (const u16*)d_in[9]; a.Bre = (const u16*)d_in[10]; a.Bim = (const u16*)d_in[11];
    a.Cre = (const u16*)d_in[12]; a.Cim = (const u16*)d_in[13]; a.logdtB = (const u16*)d_in[14]; a.DskB = (const u16*)d_in[15];
    a.out = (float*)d_out; a.ws = (unsigned char*)d_ws;
    (void)hipFuncSetAttribute((const void*)kAll, hipFuncAttributeMaxDynamicSharedMemorySize, LDS_BYTES);
    kAll<<<256, 512, LDS_BYTES, stream>>>(a);
}
```

```cpp
#include <hip/hip_runtime.h>
#include <stdint.h>
#include <type_traits>
#define GAS __attribute__((address_space(1)))
#define LAS __attribute__((address_space(3)))
typedef unsigned short u16; typedef unsigned u32; typedef unsigned long long u64;
typedef float f2 __attribute__((ext_vector_type(2)));
typedef float f4 __attribute__((ext_vector_type(4)));
typedef float f16v __attribute__((ext_vector_type(16)));
typedef short bf8 __attribute__((ext_vector_type(8)));
typedef u32 u4 __attribute__((ext_vector_type(4)));
typedef u32 u2 __attribute__((ext_vector_type(2)));
typedef u32 u16v __attribute__((ext_vector_type(16)));
typedef __bf16 b2v __attribute__((ext_vector_type(2)));
typedef const GAS unsigned char* gptr;

#define OUT_AUX_A 0
#define OUT_AUX_B 0
constexpr int SEQ = 2048, DM = 576, NS = 128;
constexpr size_t WS_E    = 0;
constexpr size_t WS_FLAG = 4096;
constexpr size_t WS_AGG  = 65536;
constexpr size_t WS_H    = 1u << 20;
constexpr int LDS_BYTES = 160 * 1024;
constexpr int SPIN_CAP = 100000;
#define HAUX 16
#define LSLEEP 1
#define CPRIO 0
#define RLX_AGENT __ATOMIC_RELAXED, __HIP_MEMORY_SCOPE_AGENT

struct Args {
    const u16 *u, *logA, *Aim, *CreA, *CimA, *logdtA, *DskA;
    const u16 *x, *Lre, *Lim, *Bre, *Bim, *Cre, *Cim, *logdtB, *DskB;
    float* out; unsigned char* ws;
};
struct KA { u16v k0, k1; u4 k2; };
#define KPTR(v, i) ((gptr)(((u64)(v)[2 * (i) + 1] << 32) | (v)[2 * (i)]))
#define P_U(ka)      KPTR((ka).k0, 0)
#define P_LOGA(ka)   KPTR((ka).k0, 1)
#define P_AIM(ka)    KPTR((ka).k0, 2)
#define P_CREA(ka)   KPTR((ka).k0, 3)
#define P_CIMA(ka)   KPTR((ka).k0, 4)
#define P_LOGDTA(ka) KPTR((ka).k0, 5)
#define P_DSKA(ka)   KPTR((ka).k0, 6)
#define P_X(ka)      KPTR((ka).k0, 7)
#define P_LRE(ka)    KPTR((ka).k1, 0)
#define P_LIM(ka)    KPTR((ka).k1, 1)
#define P_BRE(ka)    KPTR((ka).k1, 2)
#define P_BIM(ka)    KPTR((ka).k1, 3)
#define P_CRE(ka)    KPTR((ka).k1, 4)
#define P_CIM(ka)    KPTR((ka).k1, 5)
#define P_LOGDTB(ka) KPTR((ka).k1, 6)
#define P_DSKB(ka)   KPTR((ka).k1, 7)
#define P_OUT(ka)    KPTR((ka).k2, 0)
#define P_WS(ka)     KPTR((ka).k2, 1)

__device__ __forceinline__ float bf2f(u32 v) { return __uint_as_float(v << 16); }
__device__ __forceinline__ float bflo(u32 v) { return __uint_as_float(v << 16); }
__device__ __forceinline__ float bfhi(u32 v) { return __uint_as_float(v & 0xFFFF0000u); }
__device__ __forceinline__ u32 cvtpk(float lo, float hi) { f2 v = {lo, hi}; return __builtin_bit_cast(u32, __builtin_convertvector(v, b2v)); }
__device__ __forceinline__ float rbf(float f) { return __uint_as_float(cvtpk(f, f) << 16); }
__device__ __forceinline__ float fexp(float x) { return __builtin_amdgcn_exp2f(x * 1.4426950408889634f); }
__device__ __forceinline__ void bar() { asm volatile("s_barrier" ::: "memory"); }
#define WAIT_LGKM0() __builtin_amdgcn_s_waitcnt(0xC07F)
#define WAIT_VM0() __builtin_amdgcn_s_waitcnt(0x0F70)
#define FRESH() asm volatile("" ::: "memory")
template <int CTRL> __device__ __forceinline__ float dpp(float v) {
    return __builtin_bit_cast(float, __builtin_amdgcn_update_dpp(0, __builtin_bit_cast(int, v), CTRL, 0xf, 0xf, true));
}
template <int CTRL> __device__ __forceinline__ f2 dpp2(f2 v) { return (f2){dpp<CTRL>(v.x), dpp<CTRL>(v.y)}; }
__device__ __forceinline__ f2 allred16(f2 v) {
    v += dpp2<0x128>(v); v += dpp2<0x124>(v); v += dpp2<0x122>(v); v += dpp2<0x121>(v); return v;
}
#define FMA2(a, b, c) __builtin_elementwise_fma((a), (b), (c))
struct c2 { f2 r, i; };
__device__ __forceinline__ c2 cmul(c2 a, c2 b) { c2 o; o.r = FMA2(a.r, b.r, -(a.i * b.i)); o.i = FMA2(a.r, b.i, a.i * b.r); return o; }
__device__ __forceinline__ c2 cfma(c2 a, c2 b, c2 c) { c2 o; o.r = FMA2(a.r, b.r, FMA2(-a.i, b.i, c.r)); o.i = FMA2(a.r, b.i, FMA2(a.i, b.r, c.i)); return o; }
__device__ __forceinline__ void cpowf(float k, float ar2, float ai2, float& re, float& im) {
    const float e = __builtin_amdgcn_exp2f(k * ar2);
    const float t = __builtin_amdgcn_fractf(k * ai2);
    re = e * __builtin_amdgcn_cosf(t); im = e * __builtin_amdgcn_sinf(t);
}
__device__ __forceinline__ c2 cpow2(float k, f2 ar2, f2 ai2) {
    float r0, i0, r1, i1; cpowf(k, ar2.x, ai2.x, r0, i0); cpowf(k, ar2.y, ai2.y, r1, i1);
    c2 o; o.r = (f2){r0, r1}; o.i = (f2){i0, i1}; return o;
}
#define PIN2(v) asm volatile("" : "+v"((v).r), "+v"((v).i))
__device__ __forceinline__ __amdgpu_buffer_rsrc_t mkrsrc(gptr p, u32 bytes) { return __builtin_amdgcn_make_buffer_rsrc((GAS void*)p, 0, bytes, 0x00020000); }
template <int GO, int LO> __device__ __forceinline__ void glds16(u32 voff, gptr sbase, u32 slds) {
    u32 t;
    asm volatile("s_add_u32 m0, %3, %5\n\tv_add_u32 %0, %4, %1\n\tglobal_load_lds_dwordx4 %0, %2"
                 : "=&v"(t) : "v"(voff), "s"(sbase), "s"(slds), "i"(GO), "i"(LO) : "memory", "scc");
}
template <int I, int N, class F> __device__ __forceinline__ void sfor(F&& f) { if constexpr (I < N) { f(std::integral_constant<int, I>{}); sfor<I + 1, N>(f); } }

constexpr int C_KC = 16384, C_COMP = 8192, LDS_MISC = 9 * C_KC;
constexpr int M_TAG = LDS_MISC, M_PF = LDS_MISC + 16, M_CL = LDS_MISC + 20;
constexpr int NHELP = 36;
constexpr u32 NOTYET = 0xFFFFFFFFu;
typedef volatile LAS u32* lword;
__device__ __forceinline__ u32 min4(u4 v) { return min(min(v.x, v.y), min(v.z, v.w)); }
template <int SLEEP> __device__ __forceinline__ void wait_ge4(LAS unsigned char* lds, int off, u32 want) {
    for (int spin = 0; spin < 64 * SPIN_CAP; spin++) {
        const u4 v = *(volatile LAS u4*)(lds + off);
        if ((u32)__builtin_amdgcn_readfirstlane(min4(v)) >= want) break;
        if (SLEEP) __builtin_amdgcn_s_sleep(SLEEP);
    }
}
__device__ __forceinline__ void wait_ge1(LAS unsigned char* lds, int off, u32 want) {
    for (int spin = 0; spin < 64 * SPIN_CAP; spin++) {
        if ((u32)__builtin_amdgcn_readfirstlane(*(lword)(lds + off)) >= want) break;
        __builtin_amdgcn_s_sleep(1);
    }
}

__device__ __forceinline__ void b_loader(KA ka, int c, int g, int lw, int lane, LAS unsigned char* lds) {
    const gptr ws = P_WS(ka);
    u64 ev;
    asm volatile("global_load_dwordx2 %0, %1, %2 sc1" : "=v"(ev) : "v"(0u), "s"(ws + WS_E) : "memory");
    const int r8 = lane >> 3, sl = lane & 7;
    const int rw = 8 * lw + r8;
    const u32 swz = (u32)((sl ^ ((rw >> 1) & 7)) << 4);
    const u32 vW = (u32)(g * 64 + (rw & 15)) * 1152 + swz;
    const u32 vX = (u32)(c * 128 + 8 * (rw & 15) + (rw >> 4)) * 1152 + swz;
    const gptr bW = ((lw >> 1) & 1) ? P_BIM(ka) : P_BRE(ka), bX = P_X(ka);
    const u32 sl_ = (u32)lw * 1024;
    auto issue = [&](auto S) {
        constexpr int s = S.value;
        sfor<0, 4>([&](auto J) { constexpr int j = J.value; glds16<s * 128 + j * (16 * 1152), (s & 3) * 32768 + j * 4096>(vW, bW, sl_); });
        sfor<0, 4>([&](auto J) { constexpr int j = J.value; glds16<s * 128 + j * (2 * 1152), (s & 3) * 32768 + 16384 + j * 4096>(vX, bX, sl_); });
    };
    const int rc = 8 * lw + r8;
    const u32 vC = (u32)(g * 64 + rc) * 1152 + (u32)((sl ^ ((rc >> 1) & 7)) << 4);
    const gptr bCre = P_CRE(ka), bCim = P_CIM(ka);
    auto load_c = [&](auto KC) {
        constexpr int kc = KC.value;
        sfor<0, 4>([&](auto I) { constexpr int i = I.value; glds16<kc * 128 + (i & 1) * (32 * 1152), kc * C_KC + i * 4096>(vC, (i >> 1) ? bCim : bCre, sl_); });
    };
#define IC(v) std::integral_constant<int, v>{}
    issue(IC(0)); issue(IC(1));
    asm volatile("s_waitcnt vmcnt(8)" : "+v"(ev) :: "memory");
    const u64 tag = ev + 1;
    if (lw == 0) { *(volatile LAS u64*)(lds + M_TAG) = tag; *(lword)(lds + M_PF) = 0; *(lword)(lds + M_CL) = 0; }
    asm volatile("s_waitcnt lgkmcnt(0)" ::: "memory");
    bar();
    issue(IC(2)); issue(IC(3));
    sfor<0, 9>([&](auto S) {
        constexpr int s = S.value;
        if constexpr (s <= 5) asm volatile("s_waitcnt vmcnt(16)" ::: "memory");
        else if constexpr (s == 6) asm volatile("s_waitcnt vmcnt(8)" ::: "memory");
        else if constexpr (s == 7) asm volatile("s_waitcnt vmcnt(0)" ::: "memory");
        bar();
        if constexpr (s + 4 <= 8) issue(IC(s + 4 <= 8 ? s + 4 : 8));
    });
    sfor<0, 9>([&](auto KC) { load_c(KC); });
    if (lw == 0) {
        const GAS u64* fl = (const GAS u64*)(ws + WS_FLAG + (size_t)(lane < NHELP ? lane : 0) * 128);
        for (int spin = 0; spin < SPIN_CAP; spin++) {
            const u64 v = __hip_atomic_load(fl, RLX_AGENT);
            if (__all(v == tag)) break;
            __builtin_amdgcn_s_sleep(4);
        }
        *(lword)(lds + M_PF) = 1;
    }
    asm volatile("s_waitcnt vmcnt(0)" ::: "memory");
    if (lane == 0) __hip_atomic_fetch_add((LAS u32*)(lds + M_CL), 1u, __ATOMIC_RELAXED, __HIP_MEMORY_SCOPE_WORKGROUP);
}

#define WINK 12
constexpr int WIN = WINK;
__device__ __forceinline__ void b_compute(KA ka, int c, int g, int w, int lane, LAS unsigned char* lds) {
    const int n = lane & 15, q = lane >> 4;
    const int pw0 = g * 64 + 16 * w + 4 * q;
    const gptr ws = P_WS(ka);
    const int pmy = pw0 + (n & 3);
    const u32 r_lr = ((const GAS u16*)P_LRE(ka))[pmy], r_li = ((const GAS u16*)P_LIM(ka))[pmy], r_dt = ((const GAS u16*)P_LOGDTB(ka))[pmy];
    f4 acc[8][2];
#pragma unroll
    for (int t = 0; t < 8; t++) { acc[t][0] = (f4){0, 0, 0, 0}; acc[t][1] = (f4){0, 0, 0, 0}; }
    const u32 sw = (u32)(n * 128 + ((q ^ ((n >> 1) & 7)) << 4));
    const u32 aW = sw + w * 4096, aX = sw + 16384;
    bf8 fw[2][2], fx[2][8];
    auto rd = [&](int buf, u32 slotoff, int ks) {
        const u32 w_ = (aW ^ (ks << 6)) + slotoff, x_ = (aX ^ (ks << 6)) + slotoff;
        fw[buf][0] = *(const LAS bf8*)(lds + w_); fw[buf][1] = *(const LAS bf8*)(lds + w_ + 2048);
#pragma unroll
        for (int t = 0; t < 8; t++) fx[buf][t] = *(const LAS bf8*)(lds + x_ + t * 2048);
    };
    auto mm = [&](int buf) {
#pragma unroll
        for (int t = 0; t < 8; t++) {
            acc[t][0] = __builtin_amdgcn_mfma_f32_16x16x32_bf16(fw[buf][0], fx[buf][t], acc[t][0], 0, 0, 0);
            acc[t][1] = __builtin_amdgcn_mfma_f32_16x16x32_bf16(fw[buf][1], fx[buf][t], acc[t][1], 0, 0, 0);
        }
    };
    f2 ar2[2], ai2[2]; c2 Lam[2], coef[2], W1[2];
    auto params = [&]() {
        const float lr = bf2f(r_lr), li = bf2f(r_li);
        const float dt = rbf(fexp(bf2f(r_dt)));
        const float a2 = (dt * lr) * 1.4426950408889634f, b2 = (dt * li) * 0.15915494309189535f;
        float Lr, Li; cpowf(1.f, a2, b2, Lr, Li);
        const float nr = Lr - 1.f, ni = Li, den = __builtin_amdgcn_rcpf(lr * lr + li * li);
        const float cr = (nr * lr + ni * li) * den, ci = (ni * lr - nr * li) * den;
#define QB(dst, v) { dst[0] = (f2){dpp<0x00>(v), dpp<0x55>(v)}; dst[1] = (f2){dpp<0xAA>(v), dpp<0xFF>(v)}; }
        f2 t_[2];
        QB(ar2, a2); QB(ai2, b2);
        QB(t_, Lr); Lam[0].r = t_[0]; Lam[1].r = t_[1]; QB(t_, Li); Lam[0].i = t_[0]; Lam[1].i = t_[1];
        QB(t_, cr); coef[0].r = t_[0]; coef[1].r = t_[1]; QB(t_, ci); coef[0].i = t_[0]; coef[1].i = t_[1];
        W1[0] = cpow2((float)(8 * (15 - n)), ar2[0], ai2[0]); W1[1] = cpow2((float)(8 * (15 - n)), ar2[1], ai2[1]);
    };
    if (CPRIO) __builtin_amdgcn_s_setprio(CPRIO);
    bar();
    const u64 tag = *(const volatile LAS u64*)(lds + M_TAG);
    rd(0, 0, 0);
    WAIT_LGKM0();
#define ILV(NR) { _Pragma("unroll") for (int i_ = 0; i_ < NR; i_++) { __builtin_amdgcn_sched_group_barrier(0x8, 1, 0); __builtin_amdgcn_sched_group_barrier(0x100, 1, 0); } __builtin_amdgcn_sched_group_barrier(0x8, 16 - NR, 0); }
#pragma unroll
    for (int s = 0; s < 9; s++) {
        __builtin_amdgcn_sched_barrier(0);
        rd(1, (s & 3) * 32768, 1);
        mm(0);
        ILV(10);
        __builtin_amdgcn_sched_barrier(0);
        WAIT_LGKM0();
        bar();
        __builtin_amdgcn_sched_barrier(0);
        if (s < 8) rd(0, ((s + 1) & 3) * 32768, 0);
        if (s == 2) params();
        mm(1);
        if (s < 8) ILV(10);
        __builtin_amdgcn_sched_barrier(0);
        WAIT_LGKM0();
    }
    const u32 tlo = (u32)tag, thi = (u32)(tag >> 32) & 0xFFu;
    c2 z[8][2];
#pragma unroll
    for (int t = 0; t < 8; t++) {
        z[t][0].r = acc[t][0].xy; z[t][1].r = acc[t][0].zw; z[t][0].i = acc[t][1].xy; z[t][1].i = acc[t][1].zw;
    }
    c2 agg[2];
    agg[0] = z[0][0]; agg[1] = z[0][1];
#pragma unroll
    for (int t = 1; t < 8; t++) { agg[0] = cfma(Lam[0], agg[0], z[t][0]); agg[1] = cfma(Lam[1], agg[1], z[t][1]); }
    c2 S[2];
#pragma unroll
    for (int h = 0; h < 2; h++) { S[h] = cmul(W1[h], agg[h]); S[h].r = allred16(S[h].r); S[h].i = allred16(S[h].i); }
    const __amdgpu_buffer_rsrc_t rs = mkrsrc(ws, 0x7fffffff);
    {
        const float sr = (n & 2) ? ((n & 1) ? S[1].r.y : S[1].r.x) : ((n & 1) ? S[0].r.y : S[0].r.x);
        const float si = (n & 2) ? ((n & 1) ? S[1].i.y : S[1].i.x) : ((n & 1) ? S[0].i.y : S[0].i.x);
        u4 gr; gr.x = tlo; gr.y = ((__float_as_uint(sr) + 0x80u) & 0xFFFFFF00u) | thi;
        gr.z = tlo; gr.w = ((__float_as_uint(si) + 0x80u) & 0xFFFFFF00u) | thi;
        if (n < 4) __builtin_amdgcn_raw_buffer_store_b128(gr, rs, (u32)(WS_AGG + ((size_t)c * DM + pw0 + n) * 16), 0, 16);
    }
    __builtin_amdgcn_sched_barrier(0);
    const int t0 = c * 128;
    const int col = lane & 31, hh = lane >> 5;
    c2 E[2], W2[2], PW[2];
#pragma unroll
    for (int h = 0; h < 2; h++) {
        c2 P = agg[h];
        c2 m = cpow2(8.f, ar2[h], ai2[h]);
        { c2 s; s.r = dpp2<0x111>(P.r); s.i = dpp2<0x111>(P.i); P = cfma(m, s, P); } m = cmul(m, m);
        { c2 s; s.r = dpp2<0x112>(P.r); s.i = dpp2<0x112>(P.i); P = cfma(m, s, P); } m = cmul(m, m);
        { c2 s; s.r = dpp2<0x114>(P.r); s.i = dpp2<0x114>(P.i); P = cfma(m, s, P); } m = cmul(m, m);
        { c2 s; s.r = dpp2<0x118>(P.r); s.i = dpp2<0x118>(P.i); P = cfma(m, s, P); }
        E[h].r = dpp2<0x111>(P.r); E[h].i = dpp2<0x111>(P.i);
        W2[h] = cpow2((float)(128 * (15 - n)), ar2[h], ai2[h]);
        PW[h] = cmul(coef[h], cpow2((float)(8 * n), ar2[h], ai2[h]));
        E[h] = cmul(coef[h], E[h]);
#pragma unroll
        for (int t = 0; t < 8; t++) { z[t][h] = cmul(coef[h], z[t][h]); PIN2(z[t][h]); }
        PIN2(E[h]); PIN2(W2[h]); PIN2(PW[h]);
    }
    c2 X[2];
    {
        const int cp = c - 16 + n;
        const bool valid = cp >= 0;
        const u32 off = (u32)(WS_AGG + ((size_t)(valid ? cp : 0) * DM + pw0) * 16);
        u4 g0, g1, g2, g3, e0, e1, e2, e3;
        auto ldg = [&](u4& a0, u4& a1, u4& a2, u4& a3) {
            a0 = __builtin_amdgcn_raw_buffer_load_b128(rs, off, 0, 16); a1 = __builtin_amdgcn_raw_buffer_load_b128(rs, off + 16, 0, 16);
            a2 = __builtin_amdgcn_raw_buffer_load_b128(rs, off + 32, 0, 16); a3 = __builtin_amdgcn_raw_buffer_load_b128(rs, off + 48, 0, 16);
        };
        auto okg = [&](const u4& a0, const u4& a1, const u4& a2, const u4& a3) {
            const u32 bad = (a0.x ^ tlo) | (a0.z ^ tlo) | (a1.x ^ tlo) | (a1.z ^ tlo) | (a2.x ^ tlo) | (a2.z ^ tlo) | (a3.x ^ tlo) | (a3.z ^ tlo)
                | (((a0.y ^ thi) | (a0.w ^ thi) | (a1.y ^ thi) | (a1.w ^ thi) | (a2.y ^ thi) | (a2.w ^ thi) | (a3.y ^ thi) | (a3.w ^ thi)) & 0xFFu);
            return __all(!valid || bad == 0);
        };
        ldg(g0, g1, g2, g3);
        __builtin_amdgcn_s_sleep(8);
        for (int spin = 0; spin < SPIN_CAP; spin++) {
            FRESH();
            ldg(e0, e1, e2, e3);
            FRESH();
            if (okg(g0, g1, g2, g3)) break;
            ldg(g0, g1, g2, g3);
            FRESH();
            if (okg(e0, e1, e2, e3)) { g0 = e0; g1 = e1; g2 = e2; g3 = e3; break; }
        }
        const u32 msk = valid ? 0xFFFFFF00u : 0u;
        c2 Sp[2];
        Sp[0].r = (f2){__uint_as_float(g0.y & msk), __uint_as_float(g1.y & msk)}; Sp[0].i = (f2){__uint_as_float(g0.w & msk), __uint_as_float(g1.w & msk)};
        Sp[1].r = (f2){__uint_as_float(g2.y & msk), __uint_as_float(g3.y & msk)}; Sp[1].i = (f2){__uint_as_float(g2.w & msk), __uint_as_float(g3.w & msk)};
#pragma unroll
        for (int h = 0; h < 2; h++) { X[h] = cmul(W2[h], Sp[h]); X[h].r = allred16(X[h].r); X[h].i = allred16(X[h].i); }
    }
    c2 hst[2];
#pragma unroll
    for (int h = 0; h < 2; h++) hst[h] = cfma(PW[h], X[h], E[h]);
    const u32 hoff = (u32)(WS_H + ((size_t)(c * 36 + g * 4 + w) * 8) * 1024 + (q & 1) * 512 + (q >> 1) * 256 + n * 16);
    wait_ge1(lds, M_PF, 1);
#pragma unroll
    for (int t = 0; t < 8; t++) {
        hst[0] = cfma(Lam[0], hst[0], z[t][0]); hst[1] = cfma(Lam[1], hst[1], z[t][1]);
        const auto s0 = __builtin_amdgcn_permlane16_swap(cvtpk(hst[0].r.x, hst[0].r.y), cvtpk(-hst[0].i.x, -hst[0].i.y), false, false);
        const auto s1 = __builtin_amdgcn_permlane16_swap(cvtpk(hst[1].r.x, hst[1].r.y), cvtpk(-hst[1].i.x, -hst[1].i.y), false, false);
        __builtin_amdgcn_raw_buffer_store_b128((u4){s0[0], s1[0], s0[1], s1[1]}, rs, hoff + t * 1024, 0, 16);
    }
    const u32 hb = (u32)(WS_H + ((size_t)c * 36 * 8 + 2 * w) * 1024 + (col >> 4) * 1024 + hh * 256 + (col & 15) * 16);
    u4 fa[WIN][2];
    auto kcof = [&](int i) { int k = g + i; return k >= 9 ? k - 9 : k; };
    auto ldh = [&](int ci) {
        const u32 so = (u32)kcof(ci >> 2) * 32768u;
        fa[ci % WIN][0] = __builtin_amdgcn_raw_buffer_load_b128(rs, hb + (ci & 3) * 8192, so, HAUX);
        fa[ci % WIN][1] = __builtin_amdgcn_raw_buffer_load_b128(rs, hb + (ci & 3) * 8192 + 512, so, HAUX);
    };
    u32 skp[2][8];
    {
        u32 xv[2][16], dsk[2];
        const GAS u16* xp = (const GAS u16*)P_X(ka) + (size_t)(t0 + 32 * hh + 2 * w) * DM + g * 64 + col;
#pragma unroll
        for (int nt = 0; nt < 2; nt++) {
            dsk[nt] = ((const GAS u16*)P_DSKB(ka))[g * 64 + nt * 32 + col];
#pragma unroll
            for (int r = 0; r < 16; r++) xv[nt][r] = xp[(8 * (r & 3) + 64 * ((r >> 2) & 1) + (r >> 3)) * DM + nt * 32];
        }
#pragma unroll
        for (int nt = 0; nt < 2; nt++) {
            const float d = bf2f(dsk[nt]);
#pragma unroll
            for (int r = 0; r < 8; r++) { skp[nt][r] = cvtpk(bf2f(xv[nt][2 * r]) * d, bf2f(xv[nt][2 * r + 1]) * d); asm volatile("" : "+v"(skp[nt][r])); }
        }
    }
    {
        const u32 po = (u32)(WS_H + ((size_t)(c * 36 + (lane < 36 ? lane : 0)) * 8 + 7) * 1024 + 1020);
        u32 pa = __builtin_amdgcn_raw_buffer_load_b32(rs, po, 0, 16), pb;
        __builtin_amdgcn_s_sleep(6);
        for (int spin = 0; spin < SPIN_CAP; spin++) {
            FRESH();
            pb = __builtin_amdgcn_raw_buffer_load_b32(rs, po, 0, 16);
            FRESH();
            if (__all(pa != NOTYET)) break;
            pa = __builtin_amdgcn_raw_buffer_load_b32(rs, po, 0, 16);
            FRESH();
            if (__all(pb != NOTYET)) break;
        }
    }
#pragma unroll
    for (int k = 0; k < WIN; k++) ldh(k);
    wait_ge1(lds, M_CL, 4);
    f16v ac2[2];
#pragma unroll
    for (int nt = 0; nt < 2; nt++)
#pragma unroll
        for (int r = 0; r < 16; r++) ac2[nt][r] = 0.f;
    u32 ab[4];
#pragma unroll
    for (int cc = 0; cc < 4; cc++) ab[cc] = col * 128 + (((2 * cc + hh) ^ ((col >> 1) & 7)) << 4);
    bf8 fb[2][4];
    auto rdc = [&](int ci) {
        const u32 ad = ab[ci & 3] + (u32)kcof(ci >> 2) * C_KC;
#pragma unroll
        for (int j = 0; j < 4; j++) fb[ci & 1][j] = *(const LAS bf8*)(lds + ad + (j & 1) * 4096 + (j >> 1) * C_COMP);
    };
#pragma unroll
    for (int cc = 0; cc < 4; cc++) asm volatile("" : "+v"(ab[cc]));
    auto stale = [&](int ci) {
        const u4 x0 = fa[ci % WIN][0], x1 = fa[ci % WIN][1];
        return __any(max(max(max(x0.x, x0.y), max(x0.z, x0.w)), max(max(x1.x, x1.y), max(x1.z, x1.w))) == NOTYET);
    };
    auto again = [&](int ci) {
        for (int spin = 0; spin < SPIN_CAP; spin++) { FRESH(); ldh(ci); if (!stale(ci)) break; }
        WAIT_VM0();
    };
    rdc(0);
    if (__builtin_expect(stale(0), 0)) again(0);
    u32 ad = 0;
#pragma unroll
    for (int ci = 0; ci < 36; ci++) {
        __builtin_amdgcn_sched_barrier(0);
        if (ci + 1 < 36) rdc(ci + 1);
        const bf8 a0 = __builtin_bit_cast(bf8, fa[ci % WIN][0]), a1 = __builtin_bit_cast(bf8, fa[ci % WIN][1]);
        ac2[0] = __builtin_amdgcn_mfma_f32_32x32x16_bf16(a0, fb[ci & 1][0], ac2[0], 0, 0, 0);
        ac2[1] = __builtin_amdgcn_mfma_f32_32x32x16_bf16(a0, fb[ci & 1][1], ac2[1], 0, 0, 0);
        ac2[0] = __builtin_amdgcn_mfma_f32_32x32x16_bf16(a1, fb[ci & 1][2], ac2[0], 0, 0, 0);
        ac2[1] = __builtin_amdgcn_mfma_f32_32x32x16_bf16(a1, fb[ci & 1][3], ac2[1], 0, 0, 0);
        bool st = false;
        if (ci + 1 < 36) st = stale(ci + 1);
        if (ci >= 1 && ci - 1 + WIN < 36) ldh(ci - 1 + WIN);
        if (ci + 1 < 36) {
            __builtin_amdgcn_sched_group_barrier(0x100, 1, 0); __builtin_amdgcn_sched_group_barrier(0x8, 1, 0);
            if (ci >= 1 && ci - 1 + WIN < 36) __builtin_amdgcn_sched_group_barrier(0x20, 1, 0);
            __builtin_amdgcn_sched_group_barrier(0x100, 1, 0); __builtin_amdgcn_sched_group_barrier(0x8, 1, 0);
            __builtin_amdgcn_sched_group_barrier(0x100, 1, 0); __builtin_amdgcn_sched_group_barrier(0x8, 1, 0);
            if (ci >= 1 && ci - 1 + WIN < 36) __builtin_amdgcn_sched_group_barrier(0x20, 1, 0);
            __builtin_amdgcn_sched_group_barrier(0x100, 1, 0); __builtin_amdgcn_sched_group_barrier(0x8, 1, 0);
        }
        __builtin_amdgcn_sched_barrier(0);
        if (__builtin_expect(st, 0)) again(ci + 1);
    }
    (void)ad;
    const __amdgpu_buffer_rsrc_t ro = mkrsrc(P_OUT(ka), SEQ * 2 * DM * 4);
    const u32 ob = (u32)(((t0 + 32 * hh + 2 * w) * (2 * DM) + DM + g * 64 + col) * 4);
#pragma unroll
    for (int r = 0; r < 16; r++)
#pragma unroll
        for (int nt = 0; nt < 2; nt++) {
            const u32 sk = skp[nt][r >> 1];
            const float v = 2.f * ac2[nt][r] + ((r & 1) ? bfhi(sk) : bflo(sk));
            __builtin_amdgcn_raw_buffer_store_b32(__float_as_uint(v), ro, ob + nt * 128, (8 * (r & 3) + 64 * ((r >> 2) & 1) + (r >> 3)) * (2 * DM * 4), OUT_AUX_B);
        }
    if (c == 15 && g == 0 && w == 0 && lane == 0) __hip_atomic_store((GAS u64*)(ws + WS_E), tag, RLX_AGENT);
}

constexpr int UT_STRIDE = 4640;
constexpr int A_CNT = 0;
constexpr int A_UT = 128;
constexpr int A_KR = A_UT + 8 * UT_STRIDE;
constexpr int KR_BYTES = 8704, KV_BYTES = 1344, KR_B = 4160, TROW = 544;
constexpr int A_WAVE = KR_BYTES + KV_BYTES;

__device__ __forceinline__ u32 pk16(u32 lo, u32 hi) { return __builtin_amdgcn_perm(hi, lo, 0x05040100u); }

__device__ __forceinline__ void a_block(KA ka, int ga, int w, int lane, LAS unsigned char* lds) {
    const int d0 = ga * 8, d = d0 + w, tid = w * 64 + lane;
    const u32 pA = *(const GAS u32*)(P_LOGA(ka) + (d * NS + 2 * lane) * 2), pI = *(const GAS u32*)(P_AIM(ka) + (d * NS + 2 * lane) * 2);
    const u32 pR = *(const GAS u32*)(P_CREA(ka) + (d * NS + 2 * lane) * 2), pC = *(const GAS u32*)(P_CIMA(ka) + (d * NS + 2 * lane) * 2);
    const u32 r_dt = ((const GAS u16*)P_LOGDTA(ka))[d], r_dk = ((const GAS u16*)P_DSKA(ka))[d];
    LAS unsigned char* kr = lds + A_KR + w * A_WAVE;
    LAS unsigned char* kv = kr + KR_BYTES;
    if (tid == 0) *(lword)(lds + A_CNT) = 0;
    asm volatile("s_waitcnt lgkmcnt(0)" ::: "memory");
    bar();
    if (lane < 32) *(LAS u4*)(lds + A_UT + w * UT_STRIDE + lane * 16) = (u4){0, 0, 0, 0};
    if (w >= 4) {
        const int t4 = tid - 256;
        u4 ur[8];
#pragma unroll
        for (int i = 0; i < 8; i++) ur[i] = *(const GAS u4*)(P_U(ka) + ((size_t)(8 * t4 + i) * DM + d0) * 2);
#pragma unroll
        for (int j = 0; j < 4; j++) {
            *(LAS u4*)(lds + A_UT + (2 * j) * UT_STRIDE + 512 + 16 * t4) =
                (u4){__builtin_amdgcn_perm(ur[1][j], ur[0][j], 0x05040100u), __builtin_amdgcn_perm(ur[3][j], ur[2][j], 0x05040100u),
                     __builtin_amdgcn_perm(ur[5][j], ur[4][j], 0x05040100u), __builtin_amdgcn_perm(ur[7][j], ur[6][j], 0x05040100u)};
            *(LAS u4*)(lds + A_UT + (2 * j + 1) * UT_STRIDE + 512 + 16 * t4) =
                (u4){__builtin_amdgcn_perm(ur[1][j], ur[0][j], 0x07060302u), __builtin_amdgcn_perm(ur[3][j], ur[2][j], 0x07060302u),
                     __builtin_amdgcn_perm(ur[5][j], ur[4][j], 0x07060302u), __builtin_amdgcn_perm(ur[7][j], ur[6][j], 0x07060302u)};
        }
        asm volatile("s_waitcnt lgkmcnt(0)" ::: "memory");
        if (lane == 0) __hip_atomic_fetch_add((LAS u32*)(lds + A_CNT), 1u, __ATOMIC_RELAXED, __HIP_MEMORY_SCOPE_WORKGROUP);
    }
    const float dt = rbf(fexp(bf2f(r_dt))), dsk = bf2f(r_dk);
    const f2 are = {-rbf(fexp(bflo(pA))) * dt, -rbf(fexp(bfhi(pA))) * dt};
    const f2 aim = (f2){bflo(pI), bfhi(pI)} * dt;
    const f2 a2 = are * 1.4426950408889634f, b2 = aim * 0.15915494309189535f;
    c2 Cc; Cc.r = (f2){bflo(pR), bfhi(pR)} * dt * 2.f; Cc.i = (f2){bflo(pC), bfhi(pC)} * dt * 2.f;
    const c2 w1 = cpow2(1.f, a2, b2), w8 = cpow2(8.f, a2, b2), w128 = cpow2(128.f, a2, b2), w512 = cpow2(512.f, a2, b2);
    const c2 w2 = cmul(w1, w1), w16 = cmul(w8, w8);
    const c2 w3 = cmul(w2, w1), w4 = cmul(w2, w2), w24 = cmul(w16, w8), w32 = cmul(w16, w16), w256 = cmul(w128, w128);
    const c2 w6 = cmul(w4, w2), w384 = cmul(w256, w128);
    auto put = [&](int row, c2 v) { *(LAS u2*)(kr + row * TROW + lane * 8) = (u2){cvtpk(v.r.x, v.i.x), cvtpk(v.r.y, v.i.y)}; };
    const int jx = lane & 15, g4 = lane >> 4;
    const u32 fra = jx * TROW + g4 * 16;
    bf8 fr[8], fl[8];
    {
        c2 v[4];
        v[0].r = (f2){1.f, 1.f}; v[0].i = (f2){0.f, 0.f}; v[1] = w8; v[2] = w16; v[3] = w24;
#pragma unroll
        for (int k = 0; k < 4; k++) {
#pragma unroll
            for (int j = 0; j < 4; j++) { c2 t; t.r = v[j].r; t.i = -v[j].i; put(4 * k + j, t); if (k < 3) v[j] = cmul(v[j], w32); }
        }
#pragma unroll
        for (int s = 0; s < 8; s++) fr[s] = *(const LAS bf8*)(kr + fra + s * 64);
    }
    auto rdl = [&]() {
#pragma unroll
        for (int s = 0; s < 8; s++) fl[s] = *(const LAS bf8*)(kr + fra + s * 64);
        __builtin_amdgcn_sched_barrier(0);
    };
    auto tile = [&]() {
        f4 a0 = {0, 0, 0, 0}, a1 = {0, 0, 0, 0};
#pragma unroll
        for (int s = 0; s < 8; s += 2) {
            a0 = __builtin_amdgcn_mfma_f32_16x16x32_bf16(fl[s], fr[s], a0, 0, 0, 0);
            a1 = __builtin_amdgcn_mfma_f32_16x16x32_bf16(fl[s + 1], fr[s + 1], a1, 0, 0, 0);
        }
        return a0 + a1;
    };
    c2 G[4];
    G[0] = Cc; G[1] = cmul(Cc, w128); G[2] = cmul(Cc, w256); G[3] = cmul(Cc, w384);
#pragma unroll
    for (int i = 0; i < 2; i++) {
        const c2 x1 = cmul(G[i], w1), x2 = cmul(G[i], w2), x3 = cmul(G[i], w3);
        put(8 * i, G[i]); put(8 * i + 1, x1); put(8 * i + 2, x2); put(8 * i + 3, x3);
        put(8 * i + 4, cmul(G[i], w4)); put(8 * i + 5, cmul(x1, w4)); put(8 * i + 6, cmul(x2, w4)); put(8 * i + 7, cmul(x3, w4));
    }
    rdl();
    const f4 kt0 = tile();
#pragma unroll
    for (int i = 0; i < 2; i++) {
        put(4 * i, G[2 + i]); put(4 * i + 1, cmul(G[2 + i], w2)); put(4 * i + 2, cmul(G[2 + i], w4)); put(4 * i + 3, cmul(G[2 + i], w6));
    }
#pragma unroll
    for (int i = 0; i < 4; i++) { G[i] = cmul(G[i], w512); put(8 + 2 * i, G[i]); put(9 + 2 * i, cmul(G[i], w4)); }
    rdl();
    const f4 kt1 = tile();
#pragma unroll
    for (int k = 0; k < 2; k++)
#pragma unroll
        for (int i = 0; i < 4; i++) { G[i] = cmul(G[i], w512); put(4 * k + i, G[i]); }
    put(8, cmul(G[0], w512));
    rdl();
    const f4 kt2 = tile();
    {
        *(LAS u2*)(kv + 2 * (128 * (g4 >> 1) + 8 * jx + 4 * (g4 & 1))) = (u2){cvtpk(kt0[0], kt0[1]), cvtpk(kt0[2], kt0[3])};
        const int i0 = g4 < 2 ? 256 + 64 * g4 + 4 * jx : 384 + 64 * (g4 - 2) + 2 * jx, i1 = i0 + (g4 < 2 ? 2 : 32);
        *(LAS u32*)(kv + 2 * i0) = cvtpk(kt1[0], kt1[1]); *(LAS u32*)(kv + 2 * i1) = cvtpk(kt1[2], kt1[3]);
        const u32 k01 = cvtpk(kt2[0], kt2[1]), k23 = cvtpk(kt2[2], kt2[3]);
        if (g4 < 2) {
            LAS u16* p = (LAS u16*)(kv + 2 * (512 + 64 * g4 + jx));
            p[0] = (u16)k01; p[16] = (u16)(k01 >> 16); p[32] = (u16)k23; p[48] = (u16)(k23 >> 16);
        } else if (lane == 32) *(LAS u16*)(kv + 2 * 640) = (u16)k01;
    }
    {
        const LAS u16* kvp = (const LAS u16*)kv;
        u32 k[4][8];
        {
            const bool s1 = lane >= 32;
            const int base = s1 ? 256 + 4 * (lane - 32) : 8 * lane;
            const int o1[8] = {0, 0, 1, 2, 2, 2, 3, 4};
#pragma unroll
            for (int i = 0; i < 8; i++) k[0][i] = kvp[base + (s1 ? o1[i] : i)];
        }
        {
            const int base = 384 + 2 * lane;
            const u32 v0 = kvp[base], v1 = kvp[base + 1], v2 = kvp[base + 2];
            k[1][0] = k[1][1] = k[1][2] = v0; k[1][3] = k[1][4] = k[1][5] = v1; k[1][6] = k[1][7] = v2;
        }
#pragma unroll
        for (int qq = 2; qq < 4; qq++) {
            const int base = 512 + 64 * (qq - 2) + lane;
            const u32 v0 = kvp[base], v1 = kvp[base + 1];
            k[qq][0] = k[qq][1] = k[qq][2] = k[qq][3] = v0; k[qq][4] = (lane & 1) ? v1 : v0; k[qq][5] = k[qq][6] = k[qq][7] = v1;
        }
#pragma unroll
        for (int qq = 0; qq < 4; qq++) {
            const int c = 64 * qq + lane;
            const u32 prev7 = qq ? (u32)__builtin_amdgcn_readlane((int)k[qq ? qq - 1 : 0][7], 63) : 0u;
            const u32 km1 = (u32)__builtin_amdgcn_update_dpp((int)prev7, (int)k[qq][7], 0x138, 0xf, 0xf, false);
            *(LAS u4*)(kr + 16 * (255 - c)) = (u4){pk16(k[qq][7], k[qq][6]), pk16(k[qq][5], k[qq][4]), pk16(k[qq][3], k[qq][2]), pk16(k[qq][1], k[qq][0])};
            *(LAS u4*)(kr + KR_B + 16 * (255 - c)) = (u4){pk16(k[qq][6], k[qq][5]), pk16(k[qq][4], k[qq][3]), pk16(k[qq][2], k[qq][1]), pk16(k[qq][0], km1)};
        }
        if (lane < 2) { *(LAS u4*)(kr + 4096 + lane * 16) = (u4){0, 0, 0, 0}; *(LAS u4*)(kr + KR_B + 4096 + lane * 16) = (u4){0, 0, 0, 0}; }
    }
    wait_ge1(lds, A_CNT, 4);
    f4 acc[8];
#pragma unroll
    for (int t = 0; t < 8; t++) acc[t] = (f4){0, 0, 0, 0};
    {
        const int b = jx, kk = g4 >> 1, eh = g4 & 1;
        u32 kb[4];
        const u32 ka_ = (u32)(A_KR + w * A_WAVE) + ((b & 1) ? 2 * (2047 - 16 * kk + 8 * eh - b) : KR_B + 2 * (2046 - 16 * kk + 8 * eh - b)) - 3584;
#pragma unroll
        for (int j = 0; j < 4; j++) kb[j] = ka_ + 1024 * j;
        u32 ua = (u32)(A_UT + w * UT_STRIDE) + 512 + 32 * jx - 32 * kk + 16 * eh;
        bf8 fu[2][8], fk[2][8];
        auto ld = [&](int buf, bool first) {
#pragma unroll
            for (int j = 0; j < 4; j++) asm volatile("" : "+v"(kb[j]));
#pragma unroll
            for (int i = 0; i < 8; i++) {
                const LAS u32* p = (const LAS u32*)(lds + kb[(7 - i) >> 1] + ((7 - i) & 1) * 512);
                fk[buf][i] = __builtin_bit_cast(bf8, (u4){p[0], p[1], p[2], p[3]});
                fu[buf][i] = *(const LAS bf8*)(lds + ua + 512 * i);
            }
            ua -= 64;
#pragma unroll
            for (int j = 0; j < 4; j++) kb[j] -= 64;
        };
        auto mmr = [&](int buf) {
#pragma unroll
            for (int i = 0; i < 8; i++)
#pragma unroll
                for (int t = i; t < 8; t++) acc[t] = __builtin_amdgcn_mfma_f32_16x16x32_bf16(fk[buf][i], fu[buf][t - i], acc[t], 0, 0, 0);
        };
#define ILVA() { _Pragma("unroll") for (int i_ = 0; i_ < 24; i_++) { __builtin_amdgcn_sched_group_barrier(0x8, 1, 0); __builtin_amdgcn_sched_group_barrier(0x100, 1, 0); } __builtin_amdgcn_sched_group_barrier(0x8, 12, 0); }
        ld(0, true);
        WAIT_LGKM0();
#pragma unroll 1
        for (int r = 0; r < 8; r += 2) {
            __builtin_amdgcn_sched_barrier(0);
            ld(1, false); mmr(0); ILVA();
            __builtin_amdgcn_sched_barrier(0);
            WAIT_LGKM0();
            __builtin_amdgcn_sched_barrier(0);
            ld(0, false); mmr(1); ILVA();
            __builtin_amdgcn_sched_barrier(0);
            WAIT_LGKM0();
        }
    }
    {
        LAS unsigned char* pe = lds + A_UT + w * UT_STRIDE + 512 + 2 * (16 * jx + 4 * g4);
        u2 uu[8];
#pragma unroll
        for (int t = 0; t < 8; t++) uu[t] = *(const LAS u2*)(pe + 512 * t);
        const f2 dk = {dsk, dsk};
#pragma unroll
        for (int t = 0; t < 8; t++) {
            const u32 y01 = cvtpk(acc[t][0], acc[t][1]), y23 = cvtpk(acc[t][2], acc[t][3]);
            const f2 p01 = (f2){bflo(uu[t].x), bfhi(uu[t].x)} * dk, p23 = (f2){bflo(uu[t].y), bfhi(uu[t].y)} * dk;
            const u32 s01 = cvtpk(p01.x, p01.y), s23 = cvtpk(p23.x, p23.y);
            const f2 o01 = (f2){bflo(y01), bfhi(y01)} + (f2){bflo(s01), bfhi(s01)}, o23 = (f2){bflo(y23), bfhi(y23)} + (f2){bflo(s23), bfhi(s23)};
            *(LAS u2*)(pe + 512 * t) = (u2){cvtpk(o01.x, o01.y), cvtpk(o23.x, o23.y)};
        }
    }
    asm volatile("s_waitcnt lgkmcnt(0)" ::: "memory");
    bar();
    const int hf = tid & 1;
    const __amdgpu_buffer_rsrc_t ro = mkrsrc(P_OUT(ka), SEQ * 2 * DM * 4);
#pragma unroll
    for (int ps = 0; ps < 2; ps++) {
        const int rq = (tid >> 1) + 256 * ps;
        u2 v[4];
#pragma unroll
        for (int j = 0; j < 4; j++) v[j] = *(const LAS u2*)(lds + A_UT + (4 * hf + j) * UT_STRIDE + 512 + 8 * rq);
        const u32 o = (u32)(((4 * rq) * (2 * DM) + d0 + 4 * hf) * 4);
        __builtin_amdgcn_raw_buffer_store_b128((u4){v[0].x << 16, v[1].x << 16, v[2].x << 16, v[3].x << 16}, ro, o, 0, OUT_AUX_A);
        __builtin_amdgcn_raw_buffer_store_b128((u4){v[0].x & 0xFFFF0000u, v[1].x & 0xFFFF0000u, v[2].x & 0xFFFF0000u, v[3].x & 0xFFFF0000u}, ro, o + 2 * DM * 4, 0, OUT_AUX_A);
        __builtin_amdgcn_raw_buffer_store_b128((u4){v[0].y << 16, v[1].y << 16, v[2].y << 16, v[3].y << 16}, ro, o + 4 * DM * 4, 0, OUT_AUX_A);
        __builtin_amdgcn_raw_buffer_store_b128((u4){v[0].y & 0xFFFF0000u, v[1].y & 0xFFFF0000u, v[2].y & 0xFFFF0000u, v[3].y & 0xFFFF0000u}, ro, o + 6 * DM * 4, 0, OUT_AUX_A);
    }
}

__device__ __forceinline__ void helper(KA ka, int hn, int y, int hx, int hi, int nh, int w, int lane) {
    const gptr ws = P_WS(ka);
    u64 ev;
    asm volatile("global_load_dwordx2 %0, %1, %2 sc1" : "=v"(ev) : "v"(0u), "s"(ws + WS_E) : "memory");
    const int ngrp = hx ? 4 : 5, g0 = hx ? 5 : 0;
    const int nx = 512 * 6, nw = ngrp * 64 * 6;
    const int id = (hi * 8 + w) * 64 + lane, step = nh * 512;
    u32 acc = 0;
    for (int l0 = 0; l0 < nx + 2 * nw; l0 += 4 * step) {
        u32 t[4];
#pragma unroll
        for (int j = 0; j < 4; j++) {
            const int l = min(l0 + j * step + id, nx + 2 * nw - 1);
            const bool isx = l < nx, isr = l < nx + nw;
            const int m = isx ? l : isr ? l - nx : l - nx - nw;
            const int row = m / 6, kc = 3 + m % 6;
            const gptr base = isx ? P_X(ka) + (size_t)(512 * y) * 1152 : (isr ? P_BRE(ka) : P_BIM(ka)) + (size_t)(64 * g0) * 1152;
            t[j] = *(const GAS u32*)(base + row * 1152 + kc * 128);
        }
        acc ^= t[0] ^ t[1] ^ t[2] ^ t[3];
    }
    asm volatile("" :: "v"(acc));
    if (hn >= NHELP) { asm volatile("s_waitcnt vmcnt(0)" : "+v"(ev) :: "memory"); return; }
    const __amdgpu_buffer_rsrc_t rs = mkrsrc(ws, 0x7fffffff);
    const u32 ho = (u32)(WS_H + (size_t)(hn * 8 + w) * 16384 + lane * 16);
#pragma unroll
    for (int j = 0; j < 16; j++) __builtin_amdgcn_raw_buffer_store_b128((u4){NOTYET, NOTYET, NOTYET, NOTYET}, rs, ho + j * 1024, 0, 16);
    asm volatile("s_waitcnt vmcnt(0)" : "+v"(ev) :: "memory");
    bar();
    if (w == 0 && lane == 0) __hip_atomic_store((GAS u64*)(ws + WS_FLAG + (size_t)hn * 128), ev + 1, RLX_AGENT);
}

__global__ __launch_bounds__(512, 2) void kAll(Args a_unused) {
    extern __shared__ __attribute__((aligned(16))) unsigned char smem[];
    LAS unsigned char* lds = (LAS unsigned char*)smem;
    KA ka;
    asm volatile("s_load_dwordx16 %0, %3, 0x0\n\ts_load_dwordx16 %1, %3, 0x40\n\ts_load_dwordx4 %2, %3, 0x80\n\ts_waitcnt lgkmcnt(0)"
                 : "=&s"(ka.k0), "=&s"(ka.k1), "=&s"(ka.k2) : "s"(__builtin_amdgcn_kernarg_segment_ptr()));
    const int b = blockIdx.x, x = b & 7, i = b >> 3, y = x >> 1, hx = x & 1;
    const int lane = threadIdx.x & 63, w = __builtin_amdgcn_readfirstlane(threadIdx.x >> 6);
    const int nb = hx ? 16 : 20;
    if (i < nb) {
        const int c = 4 * y + (hx ? i >> 2 : i / 5), g = hx ? 5 + (i & 3) : i % 5;
        if (w >= 4) b_loader(ka, c, g, w - 4, lane, lds);
        else b_compute(ka, c, g, w, lane, lds);
    } else if (i < nb + 8 || x == 1) {
        a_block(ka, i < nb + 8 ? 8 * x + (i - nb) : 64 + (i - nb - 8), w, lane, lds);
    } else {
        helper(ka, hx ? 16 + 8 * (y - 1) + (i - nb - 8) : 4 * y + (i - nb - 8), y, hx, i - nb - 8, hx ? 8 : 4, w, lane);
    }
}

extern "C" void kernel_launch(void* const* d_in, const int* in_sizes, int n_in, void* d_out, int out_size, void* d_ws, size_t ws_size, hipStream_t stream) {
    Args a;
    a.u = (const u16*)d_in[0]; a.logA = (const u16*)d_in[1]; a.Aim = (const u16*)d_in[2]; a.CreA = (const u16*)d_in[3]; a.CimA = (const u16*)d_in[4];
    a.logdtA = (const u16*)d_in[5]; a.DskA = (const u16*)d_in[6];
    a.x = (const u16*)d_in[7]; a.Lre = (const u16*)d_in[8]; a.Lim = (const u16*)d_in[9]; a.Bre = (const u16*)d_in[10]; a.Bim = (const u16*)d_in[11];
    a.Cre = (const u16*)d_in[12]; a.Cim = (const u16*)d_in[13]; a.logdtB = (const u16*)d_in[14]; a.DskB = (const u16*)d_in[15];
    a.out = (float*)d_out; a.ws = (unsigned char*)d_ws;
    (void)hipFuncSetAttribute((const void*)kAll, hipFuncAttributeMaxDynamicSharedMemorySize, LDS_BYTES);
    kAll<<<256, 512, LDS_BYTES, stream>>>(a);
}
```

```cpp
#include <hip/hip_runtime.h>
#include <stdint.h>
#define GAS __attribute__((address_space(1)))
#define LAS __attribute__((address_space(3)))
typedef unsigned short u16; typedef unsigned u32; typedef unsigned long long u64;
typedef float f2 __attribute__((ext_vector_type(2)));
typedef float f4 __attribute__((ext_vector_type(4)));
typedef short bf8 __attribute__((ext_vector_type(8)));
typedef u32 u4 __attribute__((ext_vector_type(4)));
typedef u32 u2 __attribute__((ext_vector_type(2)));
typedef __bf16 b2v __attribute__((ext_vector_type(2)));

constexpr int SEQ = 2048, DM = 576, NS = 128;
constexpr size_t WS_AGG = 65536;
constexpr size_t WS_H   = 1u << 20;
constexpr int LDS_BYTES = 160 * 1024;

#define STAMP(i)

struct Args {
    const u16 *u, *logA, *Aim, *CreA, *CimA, *logdtA, *DskA;
    const u16 *x, *Lre, *Lim, *Bre, *Bim, *Cre, *Cim, *logdtB, *DskB;
    float* out; unsigned char* ws;
};

__device__ __forceinline__ float bf2f(u16 v) { return __uint_as_float(((u32)v) << 16); }
__device__ __forceinline__ u32 cvtpk(float lo, float hi) { f2 v = {lo, hi}; return __builtin_bit_cast(u32, __builtin_convertvector(v, b2v)); }
__device__ __forceinline__ float rbf(float f) { return __uint_as_float(cvtpk(f, f) << 16); }
__device__ __forceinline__ void bar() { asm volatile("s_barrier" ::: "memory"); }
template <int CTRL> __device__ __forceinline__ float dpp(float v) {
    return __builtin_bit_cast(float, __builtin_amdgcn_update_dpp(0, __builtin_bit_cast(int, v), CTRL, 0xf, 0xf, true));
}
template <int CTRL> __device__ __forceinline__ f2 dpp2(f2 v) { return (f2){dpp<CTRL>(v.x), dpp<CTRL>(v.y)}; }
__device__ __forceinline__ f2 allred16(f2 v) {
    v += dpp2<0x128>(v); v += dpp2<0x124>(v); v += dpp2<0x122>(v); v += dpp2<0x121>(v); return v;
}
#define FMA2(a, b, c) __builtin_elementwise_fma((a), (b), (c))
struct c2 { f2 r, i; };
__device__ __forceinline__ c2 cmul(c2 a, c2 b) { c2 o; o.r = FMA2(a.r, b.r, -(a.i * b.i)); o.i = FMA2(a.r, b.i, a.i * b.r); return o; }
__device__ __forceinline__ c2 cfma(c2 a, c2 b, c2 c) { c2 o; o.r = FMA2(a.r, b.r, FMA2(-a.i, b.i, c.r)); o.i = FMA2(a.r, b.i, FMA2(a.i, b.r, c.i)); return o; }
__device__ __forceinline__ void cpowf(float k, float ar2, float ai2, float& re, float& im) {
    float e = __builtin_amdgcn_exp2f(k * ar2);
    float t = __builtin_amdgcn_fractf(k * ai2);
    re = e * __builtin_amdgcn_cosf(t); im = e * __builtin_amdgcn_sinf(t);
}
__device__ __forceinline__ c2 cpow2(float k, f2 ar2, f2 ai2) {
    float r0, i0, r1, i1; cpowf(k, ar2.x, ai2.x, r0, i0); cpowf(k, ar2.y, ai2.y, r1, i1);
    c2 o; o.r = (f2){r0, r1}; o.i = (f2){i0, i1}; return o;
}

constexpr size_t WS_E    = 0;
constexpr size_t WS_FLAG = 4096;
constexpr int C_KC = 16512, C_COMP = 8256, C_NT = 2064;
constexpr int LDS_MISC = 9 * C_KC;
#define RLX_AGENT __ATOMIC_RELAXED, __HIP_MEMORY_SCOPE_AGENT
constexpr int SPIN_CAP = 100000;

__device__ __forceinline__ void load_c(const Args& a, int j, int lw, int lane, LAS unsigned char* lds, int kc) {
#pragma unroll
    for (int i = 0; i < 4; i++) {
        const int id = lw + 4 * i, comp = id >> 3, row = 8 * (id & 7) + (lane >> 3);
        const unsigned char* base = (const unsigned char*)(comp ? a.Cim : a.Cre);
        const u32 voff = (j * 64 + row) * 1152 + (((lane & 7) ^ ((row >> 1) & 7)) << 4);
        __builtin_amdgcn_global_load_lds((const GAS u32*)(base + kc * 128 + voff), (LAS u32*)(lds + kc * C_KC + id * 1024 + (id >> 1) * 16), 16, 0, 0);
    }
}

__device__ __forceinline__ void b_loader(const Args& a, int c, int g, int lw, int lane, LAS unsigned char* lds) {
    const int t0 = c * 128, p0 = g * 64;
    GAS u64* ep = (GAS u64*)(a.ws + WS_E);
    u64 ev;
    asm volatile("global_load_dwordx2 %0, %1, off sc1" : "=v"(ev) : "v"(ep) : "memory");
    const unsigned char* base[8]; u32 voff[8];
#pragma unroll
    for (int j = 0; j < 8; j++) {
        const int id = lw + 4 * j;
        const int rho = 8 * (id & 15) + (lane >> 3);
        int grow;
        if (j < 4) { grow = p0 + 16 * (rho >> 5) + (rho & 15); base[j] = (const unsigned char*)(((id >> 1) & 1) ? a.Bim : a.Bre); }
        else       { grow = t0 + 8 * (rho & 15) + (rho >> 4);   base[j] = (const unsigned char*)a.x; }
        voff[j] = grow * 1152 + (((lane & 7) ^ ((rho >> 1) & 7)) << 4);
    }
    auto issue = [&](int s) {
#pragma unroll
        for (int j = 0; j < 8; j++)
            __builtin_amdgcn_global_load_lds((const GAS u32*)(base[j] + s * 128 + voff[j]),
                                             (LAS u32*)(lds + (s & 3) * 32768 + (lw + 4 * j) * 1024), 16, 0, 0);
    };
    issue(0); issue(1); issue(2); issue(3);
    asm volatile("s_waitcnt vmcnt(24)" : "+v"(ev) :: "memory");
    const u64 tag = ev + 1;
    if (lw == 0 && lane == 0) *(LAS u64*)(lds + LDS_MISC) = tag;
    asm volatile("s_waitcnt lgkmcnt(0)" ::: "memory");
    bar();
#pragma unroll
    for (int s = 0; s < 9; s++) {
        if (s == 0) asm volatile("s_waitcnt vmcnt(16)" ::: "memory");
        else if (s + 2 <= 8) asm volatile("s_waitcnt vmcnt(8)" ::: "memory");
        else if (s == 7) asm volatile("s_waitcnt vmcnt(8)" ::: "memory");
        bar();
        if (s >= 1 && s + 3 <= 8) issue(s + 3);
        if (s == 6) { load_c(a, g, lw, lane, lds, 8); load_c(a, g, lw, lane, lds, 2); }
        if (s == 7) { load_c(a, g, lw, lane, lds, 3); load_c(a, g, lw, lane, lds, 4); }
        if (s == 8) { load_c(a, g, lw, lane, lds, 5); load_c(a, g, lw, lane, lds, 6); load_c(a, g, lw, lane, lds, 7); }
    }
    bar();
    load_c(a, g, lw, lane, lds, 0); load_c(a, g, lw, lane, lds, 1);
    bar();
    if (lw == 0) {
        const GAS u64* fl = (const GAS u64*)(a.ws + WS_FLAG + (size_t)(c * 9 + (lane < 9 ? lane : 0)) * 128);
        for (int spin = 0; spin < SPIN_CAP; spin++) {
            const u64 v = __hip_atomic_load(fl, RLX_AGENT);
            if (__all(v == tag)) break;
            __builtin_amdgcn_s_sleep(1);
        }
        if (c == 15 && g == 0 && lane == 0) __hip_atomic_store(ep, tag, RLX_AGENT);
    }
    asm volatile("s_waitcnt vmcnt(0)" ::: "memory");
    bar();
}

constexpr int WIN = 12;
__device__ __forceinline__ void b_compute(const Args& a, int c, int g, int w, int lane, LAS unsigned char* lds) {
    const int n = lane & 15, q = lane >> 4;
    const int pw0 = g * 64 + 16 * w + 4 * q;
    const int pmy = pw0 + (n & 3);
    const u16 r_lr = a.Lre[pmy], r_li = a.Lim[pmy], r_dt = a.logdtB[pmy];
    f4 acc[8][2];
#pragma unroll
    for (int t = 0; t < 8; t++) { acc[t][0] = (f4){0, 0, 0, 0}; acc[t][1] = (f4){0, 0, 0, 0}; }
    const u32 sw = (u32)(n * 128 + ((q ^ ((n >> 1) & 7)) << 4));
    const u32 aW = sw + w * 4096, aX = sw + 16384;
    bf8 fw[2][2], fx[2][8];
    auto rd = [&](int buf, u32 slotoff, int ks) {
        const u32 w_ = (aW ^ (ks << 6)) + slotoff, x_ = (aX ^ (ks << 6)) + slotoff;
        fw[buf][0] = *(const LAS bf8*)(lds + w_); fw[buf][1] = *(const LAS bf8*)(lds + w_ + 2048);
#pragma unroll
        for (int t = 0; t < 8; t++) fx[buf][t] = *(const LAS bf8*)(lds + x_ + t * 2048);
    };
    auto mm = [&](int buf) {
#pragma unroll
        for (int t = 0; t < 8; t++) {
            acc[t][0] = __builtin_amdgcn_mfma_f32_16x16x32_bf16(fw[buf][0], fx[buf][t], acc[t][0], 0, 0, 0);
            acc[t][1] = __builtin_amdgcn_mfma_f32_16x16x32_bf16(fw[buf][1], fx[buf][t], acc[t][1], 0, 0, 0);
        }
    };
    f2 ar2[2], ai2[2]; c2 Lam[2], coef[2], W1[2];
    auto params = [&]() {
        const float lr = bf2f(r_lr), li = bf2f(r_li);
        const float dt = rbf(__builtin_amdgcn_exp2f(bf2f(r_dt) * 1.4426950408889634f));
        const float a2 = (dt * lr) * 1.4426950408889634f, b2 = (dt * li) * 0.15915494309189535f;
        float Lr, Li; cpowf(1.f, a2, b2, Lr, Li);
        const float nr = Lr - 1.f, ni = Li, den = __builtin_amdgcn_rcpf(lr * lr + li * li);
        const float cr = (nr * lr + ni * li) * den, ci = (ni * lr - nr * li) * den;
#define QB(dst, v) { dst[0] = (f2){dpp<0x00>(v), dpp<0x55>(v)}; dst[1] = (f2){dpp<0xAA>(v), dpp<0xFF>(v)}; }
        f2 t_[2];
        QB(ar2, a2); QB(ai2, b2);
        QB(t_, Lr); Lam[0].r = t_[0]; Lam[1].r = t_[1]; QB(t_, Li); Lam[0].i = t_[0]; Lam[1].i = t_[1];
        QB(t_, cr); coef[0].r = t_[0]; coef[1].r = t_[1]; QB(t_, ci); coef[0].i = t_[0]; coef[1].i = t_[1];
        W1[0] = cpow2((float)(8 * (15 - n)), ar2[0], ai2[0]); W1[1] = cpow2((float)(8 * (15 - n)), ar2[1], ai2[1]);
    };
    bar();
    STAMP(1);
    const u64 tag = *(const LAS u64*)(lds + LDS_MISC);
    rd(0, 0, 0);
#define ILV() { _Pragma("unroll") for (int i_ = 0; i_ < 10; i_++) { __builtin_amdgcn_sched_group_barrier(0x8, 1, 0); __builtin_amdgcn_sched_group_barrier(0x100, 1, 0); } __builtin_amdgcn_sched_group_barrier(0x8, 6, 0); }
#pragma unroll
    for (int s = 0; s < 9; s++) {
        bar();
        __builtin_amdgcn_sched_barrier(0);
        rd(1, (s & 3) * 32768, 1);
        mm(0);
        ILV();
        __builtin_amdgcn_sched_barrier(0);
        if (s < 8) rd(0, ((s + 1) & 3) * 32768, 0);
        if (s == 2) params();
        mm(1);
        if (s < 8) ILV();
        __builtin_amdgcn_sched_barrier(0);
    }
    bar();
    STAMP(2);
    const u32 tlo = (u32)tag, thi = (u32)(tag >> 32) & 0xFFu;
    c2 z[8][2];
#pragma unroll
    for (int t = 0; t < 8; t++) {
        z[t][0].r = acc[t][0].xy; z[t][1].r = acc[t][0].zw; z[t][0].i = acc[t][1].xy; z[t][1].i = acc[t][1].zw;
    }
    c2 agg[2];
#pragma unroll
    for (int h = 0; h < 2; h++) agg[h] = z[0][h];
#pragma unroll
    for (int t = 1; t < 8; t++) { agg[0] = cfma(Lam[0], agg[0], z[t][0]); agg[1] = cfma(Lam[1], agg[1], z[t][1]); }
    c2 S[2];
#pragma unroll
    for (int h = 0; h < 2; h++) { S[h] = cmul(W1[h], agg[h]); S[h].r = allred16(S[h].r); S[h].i = allred16(S[h].i); }
    const __amdgpu_buffer_rsrc_t rs = __builtin_amdgcn_make_buffer_rsrc(a.ws, 0, 0x7fffffff, 0x00020000);
    {
        const float sr = (n & 2) ? ((n & 1) ? S[1].r.y : S[1].r.x) : ((n & 1) ? S[0].r.y : S[0].r.x);
        const float si = (n & 2) ? ((n & 1) ? S[1].i.y : S[1].i.x) : ((n & 1) ? S[0].i.y : S[0].i.x);
        u4 gr; gr.x = tlo; gr.y = ((__float_as_uint(sr) + 0x80u) & 0xFFFFFF00u) | thi;
        gr.z = tlo; gr.w = ((__float_as_uint(si) + 0x80u) & 0xFFFFFF00u) | thi;
        if (n < 4) __builtin_amdgcn_raw_buffer_store_b128(gr, rs, (u32)(WS_AGG + ((size_t)c * DM + pw0 + n) * 16), 0, 16);
    }
    STAMP(3);
    const int t0 = c * 128;
    u16 xv[2][4][4]; u16 dsk[4];
#pragma unroll
    for (int nt = 0; nt < 4; nt++) {
        dsk[nt] = a.DskB[g * 64 + nt * 16 + n];
#pragma unroll
        for (int m = 0; m < 2; m++)
#pragma unroll
            for (int r = 0; r < 4; r++) xv[m][nt][r] = a.x[(size_t)(t0 + 8 * (4 * q + r) + 2 * w + m) * DM + g * 64 + nt * 16 + n];
    }
    c2 E[2];
#pragma unroll
    for (int h = 0; h < 2; h++) {
        c2 P = agg[h];
        c2 m = cpow2(8.f, ar2[h], ai2[h]);
        { c2 s; s.r = dpp2<0x111>(P.r); s.i = dpp2<0x111>(P.i); P = cfma(m, s, P); } m = cmul(m, m);
        { c2 s; s.r = dpp2<0x112>(P.r); s.i = dpp2<0x112>(P.i); P = cfma(m, s, P); } m = cmul(m, m);
        { c2 s; s.r = dpp2<0x114>(P.r); s.i = dpp2<0x114>(P.i); P = cfma(m, s, P); } m = cmul(m, m);
        { c2 s; s.r = dpp2<0x118>(P.r); s.i = dpp2<0x118>(P.i); P = cfma(m, s, P); }
        E[h].r = dpp2<0x111>(P.r); E[h].i = dpp2<0x111>(P.i);
    }
    c2 W2[2], PW[2];
#pragma unroll
    for (int h = 0; h < 2; h++) {
        W2[h] = cpow2((float)(128 * (15 - n)), ar2[h], ai2[h]);
        PW[h] = cmul(coef[h], cpow2((float)(8 * n), ar2[h], ai2[h]));
        E[h] = cmul(coef[h], E[h]);
#pragma unroll
        for (int t = 0; t < 8; t++) z[t][h] = cmul(coef[h], z[t][h]);
    }
    u32 skp[2][4][2];
#pragma unroll
    for (int m = 0; m < 2; m++)
#pragma unroll
        for (int nt = 0; nt < 4; nt++) {
            const float d = bf2f(dsk[nt]);
            skp[m][nt][0] = cvtpk(bf2f(xv[m][nt][0]) * d, bf2f(xv[m][nt][1]) * d);
            skp[m][nt][1] = cvtpk(bf2f(xv[m][nt][2]) * d, bf2f(xv[m][nt][3]) * d);
        }
    c2 X[2];
    {
        const int cp = c - 16 + n;
        const bool valid = cp >= 0;
        const u32 off = (u32)(WS_AGG + ((size_t)(valid ? cp : 0) * DM + pw0) * 16);
        u4 g0, g1, g2, g3;
        for (int spin = 0; spin < SPIN_CAP; spin++) {
            g0 = __builtin_amdgcn_raw_buffer_load_b128(rs, off, 0, 16);
            g1 = __builtin_amdgcn_raw_buffer_load_b128(rs, off + 16, 0, 16);
            g2 = __builtin_amdgcn_raw_buffer_load_b128(rs, off + 32, 0, 16);
            g3 = __builtin_amdgcn_raw_buffer_load_b128(rs, off + 48, 0, 16);
            const u32 bad = (g0.x ^ tlo) | (g0.z ^ tlo) | (g1.x ^ tlo) | (g1.z ^ tlo) | (g2.x ^ tlo) | (g2.z ^ tlo) | (g3.x ^ tlo) | (g3.z ^ tlo)
                | (((g0.y ^ thi) | (g0.w ^ thi) | (g1.y ^ thi) | (g1.w ^ thi) | (g2.y ^ thi) | (g2.w ^ thi) | (g3.y ^ thi) | (g3.w ^ thi)) & 0xFFu);
            if (__all(!valid || bad == 0)) break;
            __builtin_amdgcn_s_sleep(1);
        }
        const u32 msk = valid ? 0xFFFFFF00u : 0u;
        c2 Sp[2];
        Sp[0].r = (f2){__uint_as_float(g0.y & msk), __uint_as_float(g1.y & msk)}; Sp[0].i = (f2){__uint_as_float(g0.w & msk), __uint_as_float(g1.w & msk)};
        Sp[1].r = (f2){__uint_as_float(g2.y & msk), __uint_as_float(g3.y & msk)}; Sp[1].i = (f2){__uint_as_float(g2.w & msk), __uint_as_float(g3.w & msk)};
#pragma unroll
        for (int h = 0; h < 2; h++) { X[h] = cmul(W2[h], Sp[h]); X[h].r = allred16(X[h].r); X[h].i = allred16(X[h].i); }
    }
    STAMP(4);
    c2 hst[2];
#pragma unroll
    for (int h = 0; h < 2; h++) hst[h] = cfma(PW[h], X[h], E[h]);
    const u32 hoff = (u32)(WS_H + ((size_t)(c * 36 + g * 4 + w) * 8) * 1024 + lane * 16);
#pragma unroll
    for (int t = 0; t < 8; t++) {
        hst[0] = cfma(Lam[0], hst[0], z[t][0]); hst[1] = cfma(Lam[1], hst[1], z[t][1]);
        u4 o; o.x = cvtpk(hst[0].r.x, hst[0].r.y); o.y = cvtpk(hst[1].r.x, hst[1].r.y);
        o.z = cvtpk(-hst[0].i.x, -hst[0].i.y); o.w = cvtpk(-hst[1].i.x, -hst[1].i.y);
        __builtin_amdgcn_raw_buffer_store_b128(o, rs, hoff + t * 1024, 0, 16);
    }
    asm volatile("s_waitcnt vmcnt(0)" ::: "memory");
    STAMP(5);
    bar();
    if (w == 0 && lane == 0) __hip_atomic_store((GAS u64*)(a.ws + WS_FLAG + (size_t)(c * 9 + g) * 128), tag, RLX_AGENT);
    const u32 hb = (u32)(WS_H + ((size_t)c * 36 * 8 + 2 * w) * 1024 + lane * 16);
    u4 fa[WIN][2];
    auto kcof = [&](int i) { int k = g + i; return k >= 9 ? k - 9 : k; };
    auto ldh = [&](int ci) {
        const u32 so = (u32)(kcof(ci >> 2) * 4 + (ci & 3)) * 8192u;
        fa[ci % WIN][0] = __builtin_amdgcn_raw_buffer_load_b128(rs, hb, so, 16);
        fa[ci % WIN][1] = __builtin_amdgcn_raw_buffer_load_b128(rs, hb + 1024, so, 16);
    };
#pragma unroll
    for (int k = 0; k < 4; k++) ldh(k);
    STAMP(6);
    bar();
    STAMP(7);
#pragma unroll
    for (int k = 4; k < WIN; k++) ldh(k);
    f4 ac2[2][4];
#pragma unroll
    for (int m = 0; m < 2; m++)
#pragma unroll
        for (int nt = 0; nt < 4; nt++) ac2[m][nt] = (f4){0, 0, 0, 0};
    u32 ab[4];
#pragma unroll
    for (int cc = 0; cc < 4; cc++) ab[cc] = (n >> 3) * 1024 + (n & 7) * 128 + ((((2 * cc + (q >> 1)) ^ ((n >> 1) & 7))) << 4) + (q & 1) * 8;
    bf8 fb[2][4];
    auto rdc = [&](int ci) {
        const u32 ad = ab[ci & 3] + (u32)kcof(ci >> 2) * C_KC;
#pragma unroll
        for (int nt = 0; nt < 4; nt++) {
            const u2 re = *(const LAS u2*)(lds + ad + nt * C_NT), im = *(const LAS u2*)(lds + ad + nt * C_NT + C_COMP);
            fb[ci & 1][nt] = __builtin_bit_cast(bf8, (u4){re.x, re.y, im.x, im.y});
        }
    };
    rdc(0);
#pragma unroll
    for (int ci = 0; ci < 36; ci++) {
        __builtin_amdgcn_sched_barrier(0);
        if (ci + 1 < 36) rdc(ci + 1);
        const bf8 a0 = __builtin_bit_cast(bf8, fa[ci % WIN][0]), a1 = __builtin_bit_cast(bf8, fa[ci % WIN][1]);
#pragma unroll
        for (int nt = 0; nt < 4; nt++) {
            ac2[0][nt] = __builtin_amdgcn_mfma_f32_16x16x32_bf16(a0, fb[ci & 1][nt], ac2[0][nt], 0, 0, 0);
            ac2[1][nt] = __builtin_amdgcn_mfma_f32_16x16x32_bf16(a1, fb[ci & 1][nt], ac2[1][nt], 0, 0, 0);
        }
        if (ci + 1 < 36) { _Pragma("unroll") for (int i_ = 0; i_ < 8; i_++) { __builtin_amdgcn_sched_group_barrier(0x8, 1, 0); __builtin_amdgcn_sched_group_barrier(0x100, 1, 0); } }
        __builtin_amdgcn_sched_barrier(0);
        if (ci + WIN < 36) ldh(ci + WIN);
    }
    STAMP(8);
#pragma unroll
    for (int m = 0; m < 2; m++)
#pragma unroll
        for (int r = 0; r < 4; r++)
#pragma unroll
            for (int nt = 0; nt < 4; nt++) {
                const u32 sk = skp[m][nt][r >> 1];
                a.out[(size_t)(t0 + 8 * (4 * q + r) + 2 * w + m) * (2 * DM) + DM + g * 64 + nt * 16 + n] =
                    2.f * ac2[m][nt][r] + __uint_as_float((r & 1) ? (sk & 0xFFFF0000u) : (sk << 16));
            }
    STAMP(9);
}

constexpr int UT_STRIDE = 4640;
constexpr int A_KR = 8 * UT_STRIDE;
constexpr int KR_BYTES = 8704, KV_BYTES = 1344, KR_B = 4160, TROW = 544;
constexpr int A_WAVE = KR_BYTES + KV_BYTES;

__device__ __forceinline__ u32 pk16(u32 lo, u32 hi) { return __builtin_amdgcn_perm(hi, lo, 0x05040100u); }

__device__ __forceinline__ void a_block(const Args& a, int ga, int w, int lane, LAS unsigned char* lds) {
    const int d0 = ga * 8, d = d0 + w, tid = w * 64 + lane;
    u4 ur[4];
#pragma unroll
    for (int i = 0; i < 4; i++) ur[i] = *(const u4*)(a.u + (size_t)(4 * tid + i) * DM + d0);
    const u32 pA = *(const u32*)(a.logA + d * NS + 2 * lane), pI = *(const u32*)(a.Aim + d * NS + 2 * lane);
    const u32 pR = *(const u32*)(a.CreA + d * NS + 2 * lane), pC = *(const u32*)(a.CimA + d * NS + 2 * lane);
    const float dt = rbf(expf(bf2f(a.logdtA[d]))), dsk = bf2f(a.DskA[d]);
    LAS unsigned char* kr = lds + A_KR + w * A_WAVE;
    LAS unsigned char* kv = kr + KR_BYTES;
    const f2 lA = {__uint_as_float(pA << 16), __uint_as_float(pA & 0xFFFF0000u)};
    const f2 are = {-rbf(expf(lA.x)) * dt, -rbf(expf(lA.y)) * dt};
    const f2 aim = (f2){__uint_as_float(pI << 16), __uint_as_float(pI & 0xFFFF0000u)} * dt;
    const f2 a2 = are * 1.4426950408889634f, b2 = aim * 0.15915494309189535f;
    c2 Cc; Cc.r = (f2){__uint_as_float(pR << 16), __uint_as_float(pR & 0xFFFF0000u)} * dt * 2.f;
    Cc.i = (f2){__uint_as_float(pC << 16), __uint_as_float(pC & 0xFFFF0000u)} * dt * 2.f;
    const c2 w1 = cpow2(1.f, a2, b2), w8 = cpow2(8.f, a2, b2), w128 = cpow2(128.f, a2, b2);
    const c2 w2 = cmul(w1, w1), w4 = cmul(w2, w2);
    auto put = [&](int row, c2 v) { *(LAS u2*)(kr + row * TROW + lane * 8) = (u2){cvtpk(v.r.x, v.i.x), cvtpk(v.r.y, v.i.y)}; };
    const int jx = lane & 15, g4 = lane >> 4;
    const u32 fra = jx * TROW + g4 * 16;
    bf8 fr[8];
    {
        c2 v; v.r = (f2){1.f, 1.f}; v.i = (f2){0.f, 0.f};
#pragma unroll
        for (int j = 0; j < 16; j++) { c2 t; t.r = v.r; t.i = -v.i; put(j, t); if (j < 15) v = cmul(v, w8); }
#pragma unroll
        for (int s = 0; s < 8; s++) fr[s] = *(const LAS bf8*)(kr + fra + s * 64);
    }
    auto tile = [&]() {
        f4 acc = {0, 0, 0, 0};
#pragma unroll
        for (int s = 0; s < 8; s++) acc = __builtin_amdgcn_mfma_f32_16x16x32_bf16(*(const LAS bf8*)(kr + fra + s * 64), fr[s], acc, 0, 0, 0);
        return acc;
    };
    c2 G = Cc;
    {
        asm volatile("s_waitcnt lgkmcnt(0)" ::: "memory");
#pragma unroll
        for (int i = 0; i < 2; i++) {
            c2 v = G;
#pragma unroll
            for (int e = 0; e < 8; e++) { put(8 * i + e, v); if (e < 7) v = cmul(v, w1); }
            G = cmul(G, w128);
        }
        const f4 k = tile();
        *(LAS u2*)(kv + 2 * (128 * (g4 >> 1) + 8 * jx + 4 * (g4 & 1))) = (u2){cvtpk(k[0], k[1]), cvtpk(k[2], k[3])};
    }
    {
        asm volatile("s_waitcnt lgkmcnt(0)" ::: "memory");
#pragma unroll
        for (int i = 0; i < 2; i++) {
            c2 v = G;
#pragma unroll
            for (int e = 0; e < 4; e++) { put(4 * i + e, v); if (e < 3) v = cmul(v, w2); }
            G = cmul(G, w128);
        }
#pragma unroll
        for (int i = 0; i < 4; i++) { put(8 + 2 * i, G); put(9 + 2 * i, cmul(G, w4)); G = cmul(G, w128); }
        const f4 k = tile();
        const int i0 = g4 < 2 ? 256 + 64 * g4 + 4 * jx : 384 + 64 * (g4 - 2) + 2 * jx, i1 = i0 + (g4 < 2 ? 2 : 32);
        *(LAS u32*)(kv + 2 * i0) = cvtpk(k[0], k[1]); *(LAS u32*)(kv + 2 * i1) = cvtpk(k[2], k[3]);
    }
    {
        asm volatile("s_waitcnt lgkmcnt(0)" ::: "memory");
#pragma unroll
        for (int i = 0; i < 9; i++) { put(i, G); if (i < 8) G = cmul(G, w128); }
        const f4 k = tile();
        const u32 k01 = cvtpk(k[0], k[1]), k23 = cvtpk(k[2], k[3]);
        if (g4 < 2) {
            LAS u16* p = (LAS u16*)(kv + 2 * (512 + 64 * g4 + jx));
            p[0] = (u16)k01; p[16] = (u16)(k01 >> 16); p[32] = (u16)k23; p[48] = (u16)(k23 >> 16);
        } else if (lane == 32) *(LAS u16*)(kv + 2 * 640) = (u16)k01;
    }
#pragma unroll
    for (int j = 0; j < 4; j++) {
        const u32 r0 = ur[0][j], r1 = ur[1][j], r2 = ur[2][j], r3 = ur[3][j];
        *(LAS u2*)(lds + (2 * j) * UT_STRIDE + 512 + 8 * tid) = (u2){__builtin_amdgcn_perm(r1, r0, 0x05040100u), __builtin_amdgcn_perm(r3, r2, 0x05040100u)};
        *(LAS u2*)(lds + (2 * j + 1) * UT_STRIDE + 512 + 8 * tid) = (u2){__builtin_amdgcn_perm(r1, r0, 0x07060302u), __builtin_amdgcn_perm(r3, r2, 0x07060302u)};
    }
    if (lane < 32) *(LAS u4*)(lds + w * UT_STRIDE + lane * 16) = (u4){0, 0, 0, 0};
    asm volatile("s_waitcnt lgkmcnt(0)" ::: "memory");
    {
        const LAS u16* kvp = (const LAS u16*)kv;
        u32 prev7 = 0;
#pragma unroll
        for (int qq = 0; qq < 4; qq++) {
            const int c = 64 * qq + lane;
            u32 k[8];
            if (qq == 0) {
                const bool s1 = lane >= 32;
                const int base = s1 ? 256 + 4 * (lane - 32) : 8 * lane;
                const int o1[8] = {0, 0, 1, 2, 2, 2, 3, 4};
#pragma unroll
                for (int i = 0; i < 8; i++) k[i] = kvp[base + (s1 ? o1[i] : i)];
            } else if (qq == 1) {
                const int base = 384 + 2 * lane;
                const u32 v0 = kvp[base], v1 = kvp[base + 1], v2 = kvp[base + 2];
                k[0] = k[1] = k[2] = v0; k[3] = k[4] = k[5] = v1; k[6] = k[7] = v2;
            } else {
                const int base = 512 + (c - 128);
                const u32 v0 = kvp[base], v1 = kvp[base + 1];
                k[0] = k[1] = k[2] = k[3] = v0; k[4] = (lane & 1) ? v1 : v0; k[5] = k[6] = k[7] = v1;
            }
            const u32 km1 = (u32)__builtin_amdgcn_update_dpp((int)prev7, (int)k[7], 0x138, 0xf, 0xf, false);
            prev7 = (u32)__builtin_amdgcn_readlane((int)k[7], 63);
            *(LAS u4*)(kr + 16 * (255 - c)) = (u4){pk16(k[7], k[6]), pk16(k[5], k[4]), pk16(k[3], k[2]), pk16(k[1], k[0])};
            *(LAS u4*)(kr + KR_B + 16 * (255 - c)) = (u4){pk16(k[6], k[5]), pk16(k[4], k[3]), pk16(k[2], k[1]), pk16(k[0], km1)};
        }
        if (lane < 2) { *(LAS u4*)(kr + 4096 + lane * 16) = (u4){0, 0, 0, 0}; *(LAS u4*)(kr + KR_B + 4096 + lane * 16) = (u4){0, 0, 0, 0}; }
    }
    STAMP(1);
    asm volatile("s_waitcnt lgkmcnt(0)" ::: "memory");
    bar();
    STAMP(2);
    f4 acc[8];
#pragma unroll
    for (int t = 0; t < 8; t++) acc[t] = (f4){0, 0, 0, 0};
    {
        const int b = jx, kk = g4 >> 1, eh = g4 & 1;
        u32 ka = (u32)(A_KR + w * A_WAVE) + ((b & 1) ? 2 * (2047 - 16 * kk + 8 * eh - b) : KR_B + 2 * (2046 - 16 * kk + 8 * eh - b)) - 3584;
        u32 ua = (u32)(w * UT_STRIDE) + 512 + 32 * jx - 32 * kk + 16 * eh;
        for (int r = 0; r < 8; r++) {
            bf8 fu[8];
#pragma unroll
            for (int t = 0; t < 8; t++) fu[t] = *(const LAS bf8*)(lds + ua + 512 * t);
#pragma unroll
            for (int i = 0; i < 8; i++) {
                const LAS u32* p = (const LAS u32*)(lds + ka + 512 * (7 - i));
                const bf8 fk = __builtin_bit_cast(bf8, (u4){p[0], p[1], p[2], p[3]});
#pragma unroll
                for (int t = i; t < 8; t++) acc[t] = __builtin_amdgcn_mfma_f32_16x16x32_bf16(fk, fu[t - i], acc[t], 0, 0, 0);
            }
            ka -= 64; ua -= 64;
        }
    }
    STAMP(3);
#pragma unroll
    for (int t = 0; t < 8; t++) {
        LAS u2* p = (LAS u2*)(lds + w * UT_STRIDE + 512 + 2 * (256 * t + 16 * jx + 4 * g4));
        const u2 uu = *p;
        const u32 y01 = cvtpk(acc[t][0], acc[t][1]), y23 = cvtpk(acc[t][2], acc[t][3]);
        const u32 s01 = cvtpk(__uint_as_float(uu.x << 16) * dsk, __uint_as_float(uu.x & 0xFFFF0000u) * dsk);
        const u32 s23 = cvtpk(__uint_as_float(uu.y << 16) * dsk, __uint_as_float(uu.y & 0xFFFF0000u) * dsk);
        *p = (u2){cvtpk(__uint_as_float(y01 << 16) + __uint_as_float(s01 << 16), __uint_as_float(y01 & 0xFFFF0000u) + __uint_as_float(s01 & 0xFFFF0000u)),
                  cvtpk(__uint_as_float(y23 << 16) + __uint_as_float(s23 << 16), __uint_as_float(y23 & 0xFFFF0000u) + __uint_as_float(s23 & 0xFFFF0000u))};
    }
    asm volatile("s_waitcnt lgkmcnt(0)" ::: "memory");
    bar();
    STAMP(4);
    const int hf = tid & 1;
#pragma unroll
    for (int ps = 0; ps < 2; ps++) {
        const int rq = (tid >> 1) + 256 * ps;
        u2 v[4];
#pragma unroll
        for (int j = 0; j < 4; j++) v[j] = *(const LAS u2*)(lds + (4 * hf + j) * UT_STRIDE + 512 + 8 * rq);
        float* o = a.out + (size_t)(4 * rq) * (2 * DM) + d0 + 4 * hf;
        *(f4*)(o) = (f4){__uint_as_float(v[0].x << 16), __uint_as_float(v[1].x << 16), __uint_as_float(v[2].x << 16), __uint_as_float(v[3].x << 16)};
        *(f4*)(o + 2 * DM) = (f4){__uint_as_float(v[0].x & 0xFFFF0000u), __uint_as_float(v[1].x & 0xFFFF0000u), __uint_as_float(v[2].x & 0xFFFF0000u), __uint_as_float(v[3].x & 0xFFFF0000u)};
        *(f4*)(o + 4 * DM) = (f4){__uint_as_float(v[0].y << 16), __uint_as_float(v[1].y << 16), __uint_as_float(v[2].y << 16), __uint_as_float(v[3].y << 16)};
        *(f4*)(o + 6 * DM) = (f4){__uint_as_float(v[0].y & 0xFFFF0000u), __uint_as_float(v[1].y & 0xFFFF0000u), __uint_as_float(v[2].y & 0xFFFF0000u), __uint_as_float(v[3].y & 0xFFFF0000u)};
    }
    STAMP(5);
}

__global__ __launch_bounds__(512, 2) void kAll(Args a) {
    extern __shared__ __attribute__((aligned(16))) unsigned char smem[];
    LAS unsigned char* lds = (LAS unsigned char*)smem;
    const int b = blockIdx.x, xcd = b & 7, i = b >> 3;
    const int lane = threadIdx.x & 63, w = __builtin_amdgcn_readfirstlane(threadIdx.x >> 6);
    if (w == 0) STAMP(0);
    if (i < 18) {
        const int c = 2 * xcd + (i >= 9), g = i % 9;
        if (w < 4) b_compute(a, c, g, w, lane, lds);
        else b_loader(a, c, g, w - 4, lane, lds);
    } else a_block(a, 9 * xcd + (i - 18), w, lane, lds);
}

extern "C" void kernel_launch(void* const* d_in, const int* in_sizes, int n_in, void* d_out, int out_size, void* d_ws, size_t ws_size, hipStream_t stream) {
    Args a;
    a.u = (const u16*)d_in[0]; a.logA = (const u16*)d_in[1]; a.Aim = (const u16*)d_in[2]; a.CreA = (const u16*)d_in[3]; a.CimA = (const u16*)d_in[4];
    a.logdtA = (const u16*)d_in[5]; a.DskA = (const u16*)d_in[6];
    a.x = (const u16*)d_in[7]; a.Lre = (const u16*)d_in[8]; a.Lim = (const u16*)d_in[9]; a.Bre = (const u16*)d_in[10]; a.Bim = (const u16*)d_in[11];
    a.Cre = (const u16*)d_in[12]; a.Cim = (const u16*)d_in[13]; a.logdtB = (const u16*)d_in[14]; a.DskB = (const u16*)d_in[15];
    a.out = (float*)d_out; a.ws = (unsigned char*)d_ws;
    (void)hipFuncSetAttribute((const void*)kAll, hipFuncAttributeMaxDynamicSharedMemorySize, LDS_BYTES);
    kAll<<<216, 512, LDS_BYTES, stream>>>(a);
}
```
